```python
import torch

def _softmax(x, axis=-1):
    return torch.softmax(x, dim=axis)
def _gelu(x):
    return torch.nn.functional.gelu(x, approximate="tanh")
def _silu(x):
    return torch.nn.functional.silu(x)
def _sigmoid(x):
    return torch.sigmoid(x)
def _layernorm(x, axis=-1, eps=1e-5):
    m = x.mean(dim=axis, keepdim=True)
    v = x.var(dim=axis, keepdim=True, unbiased=False)
    return (x - m) / torch.sqrt(v + eps)
def _scatter_add(tgt, idx, src, axis=0):
    return tgt.clone().index_add_(axis, idx, src)
def _segment_sum(src, seg, n):
    out = torch.zeros((n,) + src.shape[1:], dtype=src.dtype)
    return out.index_add_(0, seg, src)
def _pad(x, pw):
    flat = tuple(v for pair in reversed(pw) for v in pair)
    return torch.nn.functional.pad(x, flat)

kernel_name = 'standalone__kv_cache_quant_int4_int2_kv_with_per_channel_or_per_token_scales__sampling'

def setup_inputs():

    _w = (lambda a: (lambda t: t.to(torch.bfloat16) if t.is_floating_point() else t)(torch.as_tensor(a)))
    B = 8
    Sq = 4
    Skv = 2048
    Hq = 32
    Hkv = 16
    Dh = 128
    bits = 8
    G = 128
    if G <= 0 or G >= Skv:
        G = max(1, Skv // 2)
    Skv = (Skv // G) * G
    ngk = Skv // G
    qmax = (1 << bits) - 1
    return {
        "q": _w((lambda *s: torch.randn(*s, dtype=torch.bfloat16) * 0.4)(B, Hq, Sq, Dh)),
        "qk": _w(torch.randint(0, qmax + 1, (B, Hkv, Skv, Dh)).to(torch.int32)),
        "qv": _w(torch.randint(0, qmax + 1, (B, Hkv, Skv, Dh)).to(torch.int32)),
        "k_scales": _w(torch.abs((lambda *s: torch.randn(*s, dtype=torch.bfloat16) * 0.4)(B, Hkv, ngk, Dh)) * 0.05 + 0.02),
        "k_zeros": _w((lambda *s: torch.randn(*s, dtype=torch.bfloat16) * 0.4)(B, Hkv, ngk, Dh) + float(qmax) / 2.0),
        "v_scales": _w(torch.abs((lambda *s: torch.randn(*s, dtype=torch.bfloat16) * 0.05)(B, Hkv, Skv, 1)) * 0.05 + 0.02),
        "v_zeros": _w((lambda *s: torch.randn(*s, dtype=torch.bfloat16) * 0.05)(B, Hkv, Skv, 1) + float(qmax) / 2.0),
    }

def reference(q, qk, qv, k_scales, k_zeros, v_scales, v_zeros):
    B, Hq, Sq, Dh = q.shape
    Hkv, Skv = qk.shape[1], qk.shape[2]
    ngk = k_scales.shape[2]
    G = Skv // ngk
    ks = (lambda x, n, axis: torch.repeat_interleave(x, n, dim=axis))(k_scales, G, axis=2)
    kz = (lambda x, n, axis: torch.repeat_interleave(x, n, dim=axis))(k_zeros, G, axis=2)
    k = ((lambda x, dt: x.to(dt))(qk, q.dtype) - kz) * ks
    v = ((lambda x, dt: x.to(dt))(qv, q.dtype) - v_zeros) * v_scales
    rep = Hq // Hkv
    if rep > 1:
        k = (lambda x, n, axis: torch.repeat_interleave(x, n, dim=axis))(k, rep, axis=1)
        v = (lambda x, n, axis: torch.repeat_interleave(x, n, dim=axis))(v, rep, axis=1)
    scale = float(Dh) ** -0.5
    logits = torch.einsum("bhqd,bhkd->bhqk", q, k) * scale
    p = _softmax(logits, axis=-1)
    o = torch.einsum("bhqk,bhkd->bhqd", p, v)
    return o
```

```cpp
#include <hip/hip_runtime.h>
#include <hip/hip_bf16.h>
#include <stdint.h>

namespace {
constexpr int NB = 8, SQ = 4, SKV = 2048, HQ = 32, HKV = 16, D = 128, NG = 16, GRP = 128;
constexpr int MROWS = 8;
constexpr int WAVES = 8, NTHR = WAVES * 64;
constexpr int NSPLIT = 2;
constexpr int KEYS_WG = SKV / NSPLIT;
constexpr int TILES = KEYS_WG / (16 * WAVES);
constexpr int DEPTH = 4;

typedef unsigned v4u __attribute__((ext_vector_type(4)));
typedef unsigned v2u __attribute__((ext_vector_type(2)));
typedef float f4 __attribute__((ext_vector_type(4)));
typedef float f2 __attribute__((ext_vector_type(2)));
typedef short s8v __attribute__((ext_vector_type(8)));
typedef short s4v __attribute__((ext_vector_type(4)));
typedef __bf16 bf8v __attribute__((ext_vector_type(8)));
typedef __bf16 bf2v __attribute__((ext_vector_type(2)));

__device__ __forceinline__ unsigned pack_bf16(float lo, float hi) {
  bf2v v; v[0] = (__bf16)lo; v[1] = (__bf16)hi;
  return __builtin_bit_cast(unsigned, v);
}
__device__ __forceinline__ float bf_lo(unsigned u) { return __builtin_bit_cast(float, u << 16); }
__device__ __forceinline__ float bf_hi(unsigned u) { return __builtin_bit_cast(float, u & 0xffff0000u); }
__device__ __forceinline__ float round_bf16(float x) { return bf_lo(pack_bf16(x, x)); }

__device__ __forceinline__ float xg_max(float x) {
  auto s = __builtin_amdgcn_permlane16_swap(__float_as_uint(x), __float_as_uint(x), false, false);
  x = fmaxf(__uint_as_float(s[0]), __uint_as_float(s[1]));
  auto t = __builtin_amdgcn_permlane32_swap(__float_as_uint(x), __float_as_uint(x), false, false);
  return fmaxf(__uint_as_float(t[0]), __uint_as_float(t[1]));
}
__device__ __forceinline__ float xg_sum(float x) {
  auto s = __builtin_amdgcn_permlane16_swap(__float_as_uint(x), __float_as_uint(x), false, false);
  x = __uint_as_float(s[0]) + __uint_as_float(s[1]);
  auto t = __builtin_amdgcn_permlane32_swap(__float_as_uint(x), __float_as_uint(x), false, false);
  return __uint_as_float(t[0]) + __uint_as_float(t[1]);
}

constexpr int LDS_WAVE = 4096 + 64;
constexpr int LDS_KS = WAVES * LDS_WAVE;
constexpr int LDS_VS = LDS_KS + 8192;
constexpr int LDS_BYTES = LDS_VS + 8192;

__global__ void __launch_bounds__(NTHR, 2)
attn_split(const unsigned short* __restrict__ q, const v4u* __restrict__ qk, const v4u* __restrict__ qv,
           const unsigned short* __restrict__ ks, const unsigned short* __restrict__ kz,
           const unsigned short* __restrict__ vs, const unsigned short* __restrict__ vz,
           float* __restrict__ po, float* __restrict__ pml) {
  __shared__ __attribute__((aligned(16))) unsigned char lds[LDS_BYTES];
  const int tid = threadIdx.x, lane = tid & 63, wave = __builtin_amdgcn_readfirstlane(tid >> 6);
  const int g = lane >> 4, c = lane & 15;
  const int bid = blockIdx.x, bh = bid / NSPLIT, split = bid % NSPLIT;
  const int key0 = split * KEYS_WG;

  v4u qf[4];
  {
    const unsigned short* qrow = q + ((size_t)bh * MROWS + (c & 7)) * D + 8 * g;
#pragma unroll
    for (int n = 0; n < 4; ++n) qf[n] = *reinterpret_cast<const v4u*>(qrow + 32 * n);
  }
  const unsigned ks_raw = *reinterpret_cast<const unsigned*>(ks + ((size_t)bh * NG + split * (NG / NSPLIT) + wave) * D + 2 * lane);
  const unsigned kz_raw = *reinterpret_cast<const unsigned*>(kz + ((size_t)bh * NG + split * (NG / NSPLIT) + wave) * D + 2 * lane);
  const unsigned vs_raw = *reinterpret_cast<const unsigned*>(vs + (size_t)bh * SKV + key0 + 2 * tid);
  const unsigned vz_raw = *reinterpret_cast<const unsigned*>(vz + (size_t)bh * SKV + key0 + 2 * tid);

  const v4u* kb = qk + ((size_t)bh * SKV + key0 + 16 * wave) * 32;
  const v4u* vb = qv + ((size_t)bh * SKV + key0 + 16 * wave) * 32;
  const int koff = g * 32 + c;
  const int voff = 4 * g * 32 + c;
  v4u buf[DEPTH][8];
  auto issue = [&](int bt, v4u (&dst)[8]) {
    if (bt < TILES) {
#pragma unroll
      for (int i = 0; i < 8; ++i) dst[i] = __builtin_nontemporal_load(kb + bt * (128 * 32) + koff + (i >> 1) * 128 + (i & 1) * 16);
    } else {
#pragma unroll
      for (int i = 0; i < 8; ++i) dst[i] = __builtin_nontemporal_load(vb + (bt - TILES) * (128 * 32) + voff + (i >> 1) * 32 + (i & 1) * 16);
    }
  };
  __builtin_amdgcn_sched_barrier(0);
#pragma unroll
  for (int d = 0; d < DEPTH; ++d) { issue(d, buf[d]); __builtin_amdgcn_sched_barrier(0); }

  {
    float* kls = reinterpret_cast<float*>(lds + LDS_KS);
    const int d0 = 2 * lane, hh = d0 >> 6, cc = (d0 & 63) >> 2, e = d0 & 3;
    float* p = kls + ((wave * 2 + hh) * 16 + cc) * 8 + e;
    const float s0 = bf_lo(ks_raw), s1 = bf_hi(ks_raw), z0 = bf_lo(kz_raw), z1 = bf_hi(kz_raw);
    *reinterpret_cast<f2*>(p) = f2{s0, s1};
    *reinterpret_cast<f2*>(p + 4) = f2{-z0 * s0, -z1 * s1};
    float* vls = reinterpret_cast<float*>(lds + LDS_VS);
    const int k0 = 2 * tid;
    float* pv = vls + (k0 >> 2) * 8 + (k0 & 3);
    const float a0 = bf_lo(vs_raw), a1 = bf_hi(vs_raw), y0 = bf_lo(vz_raw), y1 = bf_hi(vz_raw);
    *reinterpret_cast<f2*>(pv) = f2{a0, a1};
    *reinterpret_cast<f2*>(pv + 4) = f2{-y0 * a0, -y1 * a1};
  }
  __syncthreads();

  unsigned char* wl = lds + wave * LDS_WAVE;
  f4 S[TILES];
#pragma unroll
  for (int t = 0; t < TILES; ++t) {
    v4u (&x)[8] = buf[t % DEPTH];
    const float* kls = reinterpret_cast<const float*>(lds + LDS_KS) + (t * 2 * 16 + c) * 8;
    f4 sc[2], nz[2];
#pragma unroll
    for (int h = 0; h < 2; ++h) { sc[h] = *reinterpret_cast<const f4*>(kls + h * 128); nz[h] = *reinterpret_cast<const f4*>(kls + h * 128 + 4); }
#pragma unroll
    for (int i = 0; i < 8; ++i) {
      const int j = i >> 1, h = i & 1;
      const int r = 4 * j + g;
      float f[4];
#pragma unroll
      for (int e = 0; e < 4; ++e) f[e] = fmaf((float)(int)x[i][e], sc[h][e], nz[h][e]);
      v2u w; w[0] = pack_bf16(f[0], f[1]); w[1] = pack_bf16(f[2], f[3]);
      const int chunk = (8 * h + (c >> 1)) ^ r;
      *reinterpret_cast<v2u*>(wl + r * 256 + chunk * 16 + (c & 1) * 8) = w;
    }
    __builtin_amdgcn_sched_barrier(0);
    if (t + DEPTH < 2 * TILES) issue(t + DEPTH, buf[t % DEPTH]);
    __builtin_amdgcn_sched_barrier(0);
    __builtin_amdgcn_fence(__ATOMIC_RELEASE, "wavefront");
    __builtin_amdgcn_wave_barrier();
    __builtin_amdgcn_fence(__ATOMIC_ACQUIRE, "wavefront");
    f4 acc = {0.f, 0.f, 0.f, 0.f};
#pragma unroll
    for (int n = 0; n < 4; ++n) {
      const v4u a = *reinterpret_cast<const v4u*>(wl + c * 256 + (((4 * n + g) ^ c) * 16));
      acc = __builtin_amdgcn_mfma_f32_16x16x32_bf16(__builtin_bit_cast(bf8v, a), __builtin_bit_cast(bf8v, qf[n]), acc, 0, 0, 0);
    }
    S[t] = acc;
    __builtin_amdgcn_fence(__ATOMIC_RELEASE, "wavefront");
    __builtin_amdgcn_wave_barrier();
    __builtin_amdgcn_fence(__ATOMIC_ACQUIRE, "wavefront");
  }
  constexpr float SCALE = 0.08838834764831845f;
  constexpr float LOG2E = 1.4426950408889634f;
  float m = -INFINITY;
#pragma unroll
  for (int t = 0; t < TILES; ++t)
#pragma unroll
    for (int r = 0; r < 4; ++r) {
      float s = round_bf16(round_bf16(S[t][r]) * SCALE) * LOG2E;
      S[t][r] = s;
      m = fmaxf(m, s);
    }
  m = xg_max(m);
  float l = 0.f;
  v2u P[TILES];
#pragma unroll
  for (int t = 0; t < TILES; ++t) {
    float p[4];
#pragma unroll
    for (int r = 0; r < 4; ++r) { p[r] = __builtin_amdgcn_exp2f(S[t][r] - m); l += p[r]; }
    P[t][0] = pack_bf16(p[0], p[1]); P[t][1] = pack_bf16(p[2], p[3]);
  }
  l = xg_sum(l);
  f4 O[8];
#pragma unroll
  for (int k = 0; k < 8; ++k) O[k] = f4{0.f, 0.f, 0.f, 0.f};
#pragma unroll
  for (int t = 0; t < TILES; ++t) {
    v4u (&x)[8] = buf[(t + TILES) % DEPTH];
    const float* vls = reinterpret_cast<const float*>(lds + LDS_VS) + (t * 32 + 4 * wave + g) * 8;
    const f4 sc = *reinterpret_cast<const f4*>(vls), nz = *reinterpret_cast<const f4*>(vls + 4);
    v2u a[8];
#pragma unroll
    for (int h = 0; h < 2; ++h)
#pragma unroll
      for (int e = 0; e < 4; ++e) {
        float f[4];
#pragma unroll
        for (int j = 0; j < 4; ++j) f[j] = fmaf((float)(int)x[2 * j + h][e], sc[j], nz[j]);
        a[h * 4 + e][0] = pack_bf16(f[0], f[1]); a[h * 4 + e][1] = pack_bf16(f[2], f[3]);
      }
    __builtin_amdgcn_sched_barrier(0);
    if (t + TILES + DEPTH < 2 * TILES) issue(t + TILES + DEPTH, buf[(t + TILES) % DEPTH]);
    __builtin_amdgcn_sched_barrier(0);
#pragma unroll
    for (int k = 0; k < 8; ++k)
      O[k] = __builtin_amdgcn_mfma_f32_16x16x16bf16_1k(__builtin_bit_cast(s4v, a[k]), __builtin_bit_cast(s4v, P[t]), O[k], 0, 0, 0);
  }
  {
    float* ow = reinterpret_cast<float*>(wl) + (c & 7) * 128 + (c >> 3) * 64 + 16 * g;
#pragma unroll
    for (int r = 0; r < 4; ++r) {
      f4 v;
#pragma unroll
      for (int e = 0; e < 4; ++e) v[e] = (c < 8) ? O[e][r] : O[4 + e][r];
      *reinterpret_cast<f4*>(ow + 4 * r) = v;
    }
    if (lane < 8) *reinterpret_cast<f2*>(wl + 4096 + lane * 8) = f2{m, l};
  }
  __syncthreads();
  {
    const int qr = wave, d0 = 2 * lane;
    float M = -INFINITY;
    f2 ml[WAVES];
#pragma unroll
    for (int w = 0; w < WAVES; ++w) { ml[w] = *reinterpret_cast<const f2*>(lds + w * LDS_WAVE + 4096 + qr * 8); M = fmaxf(M, ml[w][0]); }
    float L = 0.f, o0 = 0.f, o1 = 0.f;
#pragma unroll
    for (int w = 0; w < WAVES; ++w) {
      const float wt = __builtin_amdgcn_exp2f(ml[w][0] - M);
      const f2 o = *reinterpret_cast<const f2*>(lds + w * LDS_WAVE + (qr * 128 + d0) * 4);
      L = fmaf(wt, ml[w][1], L); o0 = fmaf(wt, o[0], o0); o1 = fmaf(wt, o[1], o1);
    }
    const size_t slot = ((size_t)bh * MROWS + qr) * NSPLIT + split;
    *reinterpret_cast<f2*>(po + slot * D + d0) = f2{o0, o1};
    if (lane == 0) *reinterpret_cast<f2*>(pml + 2 * slot) = f2{M, L};
  }
}

__global__ void __launch_bounds__(256) attn_combine(const float* __restrict__ po, const float* __restrict__ pml, unsigned* __restrict__ out) {
  const int row = blockIdx.x * 4 + (threadIdx.x >> 6), lane = threadIdx.x & 63;
  f2 ml[NSPLIT]; f2 o[NSPLIT];
#pragma unroll
  for (int s = 0; s < NSPLIT; ++s) {
    ml[s] = *reinterpret_cast<const f2*>(pml + 2 * ((size_t)row * NSPLIT + s));
    o[s] = *reinterpret_cast<const f2*>(po + ((size_t)row * NSPLIT + s) * D + 2 * lane);
  }
  float M = -INFINITY;
#pragma unroll
  for (int s = 0; s < NSPLIT; ++s) M = fmaxf(M, ml[s][0]);
  float L = 0.f, o0 = 0.f, o1 = 0.f;
#pragma unroll
  for (int s = 0; s < NSPLIT; ++s) {
    const float wt = __builtin_amdgcn_exp2f(ml[s][0] - M);
    L = fmaf(wt, ml[s][1], L); o0 = fmaf(wt, o[s][0], o0); o1 = fmaf(wt, o[s][1], o1);
  }
  const float inv = 1.f / L;
  out[(size_t)row * (D / 2) + lane] = pack_bf16(o0 * inv, o1 * inv);
}
}

extern "C" void kernel_launch(void* const* d_in, const int* in_sizes, int n_in, void* d_out, int out_size, void* d_ws, size_t ws_size,
                              hipStream_t stream) {
  const unsigned short* q = (const unsigned short*)d_in[0];
  const v4u* qk = (const v4u*)d_in[1];
  const v4u* qv = (const v4u*)d_in[2];
  const unsigned short* ks = (const unsigned short*)d_in[3];
  const unsigned short* kz = (const unsigned short*)d_in[4];
  const unsigned short* vs = (const unsigned short*)d_in[5];
  const unsigned short* vz = (const unsigned short*)d_in[6];
  float* pml = (float*)d_ws;
  float* po = (float*)((char*)d_ws + 65536);
  attn_split<<<NB * HKV * NSPLIT, NTHR, 0, stream>>>(q, qk, qv, ks, kz, vs, vz, po, pml);
  attn_combine<<<NB * HQ * SQ / 4, 256, 0, stream>>>(po, pml, (unsigned*)d_out);
}
```

```cpp
#include <hip/hip_runtime.h>
#include <hip/hip_bf16.h>
#include <stdint.h>

#define FUSED 1
#define UNROLL_ALL 0

namespace {
constexpr int NB = 8, SQ = 4, SKV = 2048, HQ = 32, HKV = 16, D = 128, NG = 16;
constexpr int MROWS = 8;
constexpr int WAVES = 4, NTHR = WAVES * 64;
constexpr int NSPLIT = 2;
constexpr int KEYS_WG = SKV / NSPLIT;
constexpr int TILES = KEYS_WG / (16 * WAVES);
constexpr int DEPTH = 4;
constexpr unsigned KV_BYTES = (unsigned)NB * HKV * SKV * D * 4u;
constexpr unsigned OOB = 0x80000000u;

typedef unsigned v4u __attribute__((ext_vector_type(4)));
typedef unsigned v2u __attribute__((ext_vector_type(2)));
typedef float f4 __attribute__((ext_vector_type(4)));
typedef float f2 __attribute__((ext_vector_type(2)));
typedef short s4v __attribute__((ext_vector_type(4)));
typedef __bf16 bf8v __attribute__((ext_vector_type(8)));
typedef __bf16 bf2v __attribute__((ext_vector_type(2)));
typedef unsigned long long u64;
typedef __attribute__((address_space(1))) u64 gu64;

__device__ __forceinline__ unsigned pack_bf16(float lo, float hi) {
  bf2v v; v[0] = (__bf16)lo; v[1] = (__bf16)hi;
  return __builtin_bit_cast(unsigned, v);
}
__device__ __forceinline__ float bf_lo(unsigned u) { return __builtin_bit_cast(float, u << 16); }
__device__ __forceinline__ float bf_hi(unsigned u) { return __builtin_bit_cast(float, u & 0xffff0000u); }
__device__ __forceinline__ float round_bf16(float x) { return bf_lo(pack_bf16(x, x)); }

__device__ __forceinline__ float xg_max(float x) {
  auto s = __builtin_amdgcn_permlane16_swap(__float_as_uint(x), __float_as_uint(x), false, false);
  x = fmaxf(__uint_as_float(s[0]), __uint_as_float(s[1]));
  auto t = __builtin_amdgcn_permlane32_swap(__float_as_uint(x), __float_as_uint(x), false, false);
  return fmaxf(__uint_as_float(t[0]), __uint_as_float(t[1]));
}
__device__ __forceinline__ float xg_sum(float x) {
  auto s = __builtin_amdgcn_permlane16_swap(__float_as_uint(x), __float_as_uint(x), false, false);
  x = __uint_as_float(s[0]) + __uint_as_float(s[1]);
  auto t = __builtin_amdgcn_permlane32_swap(__float_as_uint(x), __float_as_uint(x), false, false);
  return __uint_as_float(t[0]) + __uint_as_float(t[1]);
}
__device__ __forceinline__ void wave_sync() {
  __builtin_amdgcn_fence(__ATOMIC_RELEASE, "wavefront");
  __builtin_amdgcn_wave_barrier();
  __builtin_amdgcn_fence(__ATOMIC_ACQUIRE, "wavefront");
}
__device__ __forceinline__ u64 rfl64(u64 x) {
  const unsigned lo = __builtin_amdgcn_readfirstlane((unsigned)x), hi = __builtin_amdgcn_readfirstlane((unsigned)(x >> 32));
  return ((u64)hi << 32) | lo;
}

constexpr int LDS_T = 0;
constexpr int LDS_ML = 4096;
constexpr int LDS_S = 4096 + 64;
constexpr int LDS_WAVE = LDS_S + TILES * 1024;
constexpr int LDS_KS = WAVES * LDS_WAVE;
constexpr int LDS_VS = LDS_KS + 8192;
constexpr int LDS_BYTES = LDS_VS + 8192;

constexpr size_t WS_CTL = 0;
constexpr size_t WS_PML = 65536;
constexpr size_t WS_PO = 131072;

__global__ void __launch_bounds__(NTHR, 1)
attn_split(const unsigned short* __restrict__ q, const int* __restrict__ qk, const int* __restrict__ qv,
           const unsigned short* __restrict__ ks, const unsigned short* __restrict__ kz,
           const unsigned short* __restrict__ vs, const unsigned short* __restrict__ vz,
           unsigned char* __restrict__ ws, unsigned* __restrict__ out
           ) {
  __shared__ __attribute__((aligned(16))) unsigned char lds[LDS_BYTES];
  const int tid = threadIdx.x, lane = tid & 63, wave = __builtin_amdgcn_readfirstlane(tid >> 6);
  const int g = lane >> 4, c = lane & 15;
  const int bid = blockIdx.x, bh = bid / NSPLIT, split = bid % NSPLIT;
  const int key0 = split * KEYS_WG;

  v4u qf[4];
  {
    const unsigned short* qrow = q + ((size_t)bh * MROWS + (c & 7)) * D + 8 * g;
#pragma unroll
    for (int n = 0; n < 4; ++n) qf[n] = __builtin_nontemporal_load(reinterpret_cast<const v4u*>(qrow + 32 * n));
  }
  const size_t kso = ((size_t)bh * NG + split * (NG / NSPLIT)) * D + 4 * tid;
  const v2u ks_raw = __builtin_nontemporal_load(reinterpret_cast<const v2u*>(ks + kso));
  const v2u kz_raw = __builtin_nontemporal_load(reinterpret_cast<const v2u*>(kz + kso));
  const size_t vso = (size_t)bh * SKV + key0 + 4 * tid;
  const v2u vs_raw = __builtin_nontemporal_load(reinterpret_cast<const v2u*>(vs + vso));
  const v2u vz_raw = __builtin_nontemporal_load(reinterpret_cast<const v2u*>(vz + vso));
  gu64* ctl = (gu64*)(ws + WS_CTL + (size_t)(bh * WAVES + wave) * 64);
  const u64 e_raw = __hip_atomic_load(ctl, __ATOMIC_RELAXED, __HIP_MEMORY_SCOPE_AGENT);

  const __amdgpu_buffer_rsrc_t rk = __builtin_amdgcn_make_buffer_rsrc((void*)qk, 0, KV_BYTES, 0x00020000);
  const __amdgpu_buffer_rsrc_t rv = __builtin_amdgcn_make_buffer_rsrc((void*)qv, 0, KV_BYTES, 0x00020000);
  const unsigned sbase = ((unsigned)bh * SKV + key0 + 16 * wave) * 512u;
  const unsigned voff = (unsigned)(4 * g) * 512u + (unsigned)c * 16u;
  v4u buf[DEPTH][8];
  auto issue = [&](const __amdgpu_buffer_rsrc_t& r, unsigned so, v4u (&dst)[8]) {
#pragma unroll
    for (int i = 0; i < 8; ++i) dst[i] = __builtin_amdgcn_raw_buffer_load_b128(r, voff + (i >> 1) * 512 + (i & 1) * 256, so, 2);
  };
  __builtin_amdgcn_sched_barrier(0);
#pragma unroll
  for (int d = 0; d < DEPTH; ++d) { issue(rk, sbase + d * (64 * 512), buf[d]); __builtin_amdgcn_sched_barrier(0); }

  {
    f4 s, n;
    s[0] = bf_lo(ks_raw[0]); s[1] = bf_hi(ks_raw[0]); s[2] = bf_lo(ks_raw[1]); s[3] = bf_hi(ks_raw[1]);
    n[0] = -bf_lo(kz_raw[0]) * s[0]; n[1] = -bf_hi(kz_raw[0]) * s[1]; n[2] = -bf_lo(kz_raw[1]) * s[2]; n[3] = -bf_hi(kz_raw[1]) * s[3];
    *reinterpret_cast<f4*>(lds + LDS_KS + tid * 32) = s;
    *reinterpret_cast<f4*>(lds + LDS_KS + tid * 32 + 16) = n;
    s[0] = bf_lo(vs_raw[0]); s[1] = bf_hi(vs_raw[0]); s[2] = bf_lo(vs_raw[1]); s[3] = bf_hi(vs_raw[1]);
    n[0] = -bf_lo(vz_raw[0]) * s[0]; n[1] = -bf_hi(vz_raw[0]) * s[1]; n[2] = -bf_lo(vz_raw[1]) * s[2]; n[3] = -bf_hi(vz_raw[1]) * s[3];
    *reinterpret_cast<f4*>(lds + LDS_VS + tid * 32) = s;
    *reinterpret_cast<f4*>(lds + LDS_VS + tid * 32 + 16) = n;
  }
  const u64 epoch = rfl64(e_raw);
  __syncthreads();

  unsigned char* wl = lds + wave * LDS_WAVE;
  constexpr float SCALE = 0.08838834764831845f;
  constexpr float LOG2E = 1.4426950408889634f;
  float m = -INFINITY;
#pragma unroll 1
  for (int t4 = 0; t4 < TILES; t4 += DEPTH) {
    const bool last = t4 + DEPTH >= TILES;
    const __amdgpu_buffer_rsrc_t rn = last ? rv : rk;
    const unsigned sonext = sbase + (last ? 0u : (unsigned)(t4 + DEPTH) * (64 * 512));
#pragma unroll
    for (int s = 0; s < DEPTH; ++s) {
      const int t = t4 + s;
      v4u (&x)[8] = buf[s];
      const unsigned char* kls = lds + LDS_KS + ((t >> 1) * 32 + c) * 32;
      f4 sc[2], nz[2];
#pragma unroll
      for (int h = 0; h < 2; ++h) { sc[h] = *reinterpret_cast<const f4*>(kls + h * 512); nz[h] = *reinterpret_cast<const f4*>(kls + h * 512 + 16); }
#pragma unroll
      for (int i = 0; i < 8; ++i) {
        const int j = i >> 1, h = i & 1;
        const int r = 4 * g + j;
        float f[4];
#pragma unroll
        for (int e = 0; e < 4; ++e) f[e] = fmaf((float)(int)x[i][e], sc[h][e], nz[h][e]);
        v2u w; w[0] = pack_bf16(f[0], f[1]); w[1] = pack_bf16(f[2], f[3]);
        const int chunk = (8 * h + (c >> 1)) ^ r;
        *reinterpret_cast<v2u*>(wl + LDS_T + r * 256 + chunk * 16 + (c & 1) * 8) = w;
      }
      __builtin_amdgcn_sched_barrier(0);
      issue(rn, sonext + s * (64 * 512), buf[s]);
      __builtin_amdgcn_sched_barrier(0);
      wave_sync();
      f4 acc = {0.f, 0.f, 0.f, 0.f};
#pragma unroll
      for (int n = 0; n < 4; ++n) {
        const v4u a = *reinterpret_cast<const v4u*>(wl + LDS_T + c * 256 + (((4 * n + g) ^ c) * 16));
        acc = __builtin_amdgcn_mfma_f32_16x16x32_bf16(__builtin_bit_cast(bf8v, a), __builtin_bit_cast(bf8v, qf[n]), acc, 0, 0, 0);
      }
#pragma unroll
      for (int r = 0; r < 4; ++r) { acc[r] = round_bf16(round_bf16(acc[r]) * SCALE) * LOG2E; m = fmaxf(m, acc[r]); }
      *reinterpret_cast<f4*>(wl + LDS_S + t * 1024 + lane * 16) = acc;
      wave_sync();
    }
  }
  m = xg_max(m);
  float l = 0.f;
  f4 O[8];
#pragma unroll
  for (int k = 0; k < 8; ++k) O[k] = f4{0.f, 0.f, 0.f, 0.f};
  auto vtile = [&](int t, v4u (&x)[8], bool refill) {
      const f4 sv = *reinterpret_cast<const f4*>(wl + LDS_S + t * 1024 + lane * 16);
      const unsigned char* vls = lds + LDS_VS + (t * 16 + 4 * wave + g) * 32;
      const f4 sc = *reinterpret_cast<const f4*>(vls), nz = *reinterpret_cast<const f4*>(vls + 16);
      float p[4];
#pragma unroll
      for (int r = 0; r < 4; ++r) { p[r] = __builtin_amdgcn_exp2f(sv[r] - m); l += p[r]; }
      v2u P; P[0] = pack_bf16(p[0], p[1]); P[1] = pack_bf16(p[2], p[3]);
      v2u a[8];
#pragma unroll
      for (int h = 0; h < 2; ++h)
#pragma unroll
        for (int e = 0; e < 4; ++e) {
          float f[4];
#pragma unroll
          for (int j = 0; j < 4; ++j) f[j] = fmaf((float)(int)x[2 * j + h][e], sc[j], nz[j]);
          a[h * 4 + e][0] = pack_bf16(f[0], f[1]); a[h * 4 + e][1] = pack_bf16(f[2], f[3]);
        }
      if (refill) {
        asm volatile("" : "+v"(a[0]), "+v"(a[1]), "+v"(a[2]), "+v"(a[3]), "+v"(a[4]), "+v"(a[5]), "+v"(a[6]), "+v"(a[7]) : : "memory");
        __builtin_amdgcn_sched_barrier(0);
        issue(rv, sbase + (unsigned)(t + DEPTH) * (64 * 512), x);
        __builtin_amdgcn_sched_barrier(0);
      }
#pragma unroll
      for (int k = 0; k < 8; ++k)
        O[k] = __builtin_amdgcn_mfma_f32_16x16x16bf16_1k(__builtin_bit_cast(s4v, a[k]), __builtin_bit_cast(s4v, P), O[k], 0, 0, 0);
  };
#pragma unroll 1
  for (int t4 = 0; t4 < TILES - DEPTH; t4 += DEPTH) {
#pragma unroll
    for (int s = 0; s < DEPTH; ++s) vtile(t4 + s, buf[s], true);
  }
  const u64 peek_raw = __hip_atomic_load(ctl + 1, __ATOMIC_RELAXED, __HIP_MEMORY_SCOPE_AGENT);
  __builtin_amdgcn_sched_barrier(0);
#pragma unroll
  for (int s = 0; s < DEPTH; ++s) { vtile(TILES - DEPTH + s, buf[s], false); __builtin_amdgcn_sched_barrier(0); }
  l = xg_sum(l);
  const int qr = 2 * wave + (lane >> 5), d0 = 4 * (lane & 31);
  const unsigned row = (unsigned)bh * MROWS + qr;
  const __amdgpu_buffer_rsrc_t rw = __builtin_amdgcn_make_buffer_rsrc((void*)ws, 0, 4u << 20, 0x00020000);
  const unsigned po_me = (unsigned)WS_PO + ((row * NSPLIT + split) * D + d0) * 4u, po_ot = (unsigned)WS_PO + ((row * NSPLIT + (split ^ 1)) * D + d0) * 4u;
  const unsigned ml_me = (unsigned)WS_PML + (row * NSPLIT + split) * 8u, ml_ot = (unsigned)WS_PML + (row * NSPLIT + (split ^ 1)) * 8u;
  bool have = rfl64(peek_raw) == epoch + 2;
  v4u po4 = __builtin_amdgcn_raw_buffer_load_b128(rw, po_ot, have ? 0u : OOB, 16);
  v2u pml2 = __builtin_amdgcn_raw_buffer_load_b64(rw, ml_ot, have ? 0u : OOB, 16);
  __builtin_amdgcn_sched_barrier(0);
  {
    float* ow = reinterpret_cast<float*>(wl + LDS_T) + (c & 7) * 128 + (c >> 3) * 64 + 16 * g;
#pragma unroll
    for (int r = 0; r < 4; ++r) {
      f4 v;
#pragma unroll
      for (int e = 0; e < 4; ++e) v[e] = (c < 8) ? O[e][r] : O[4 + e][r];
      *reinterpret_cast<f4*>(ow + 4 * r) = v;
    }
    if (lane < 8) *reinterpret_cast<f2*>(wl + LDS_ML + lane * 8) = f2{m, l};
  }
  __syncthreads();
  float M = -INFINITY, L = 0.f;
  f4 o = {0.f, 0.f, 0.f, 0.f};
  {
    f2 ml[WAVES];
#pragma unroll
    for (int w = 0; w < WAVES; ++w) { ml[w] = *reinterpret_cast<const f2*>(lds + w * LDS_WAVE + LDS_ML + qr * 8); M = fmaxf(M, ml[w][0]); }
#pragma unroll
    for (int w = 0; w < WAVES; ++w) {
      const float wt = __builtin_amdgcn_exp2f(ml[w][0] - M);
      const f4 ov = *reinterpret_cast<const f4*>(lds + w * LDS_WAVE + LDS_T + (qr * 128 + d0) * 4);
      L = fmaf(wt, ml[w][1], L);
#pragma unroll
      for (int e = 0; e < 4; ++e) o[e] = fmaf(wt, ov[e], o[e]);
    }
  }
  bool fin = true;
  if (!have) {
    __builtin_amdgcn_raw_buffer_store_b128(__builtin_bit_cast(v4u, o), rw, po_me, 0, 16);
    __builtin_amdgcn_raw_buffer_store_b64(__builtin_bit_cast(v2u, f2{M, L}), rw, (lane & 31) == 0 ? ml_me : OOB, 0, 16);
    u64 old = 0;
    if (lane == 0) old = __hip_atomic_exchange(ctl + 1, epoch + 1, __ATOMIC_RELAXED, __HIP_MEMORY_SCOPE_AGENT);
    asm volatile("s_waitcnt vmcnt(0)" ::: "memory");
    old = rfl64(old);
    if (old == epoch + 1 || old == epoch + 2) {
      while (old != epoch + 2) {
        __builtin_amdgcn_s_sleep(2);
        old = rfl64(__hip_atomic_load(ctl + 1, __ATOMIC_RELAXED, __HIP_MEMORY_SCOPE_AGENT));
      }
      asm volatile("" ::: "memory");
      po4 = __builtin_amdgcn_raw_buffer_load_b128(rw, po_ot, 0, 16);
      pml2 = __builtin_amdgcn_raw_buffer_load_b64(rw, ml_ot, 0, 16);
    } else {
      if (lane == 0) __hip_atomic_store(ctl + 1, epoch + 2, __ATOMIC_RELAXED, __HIP_MEMORY_SCOPE_AGENT);
      fin = false;
    }
  }
  if (fin) {
    const float M2 = __uint_as_float(pml2[0]), L2 = __uint_as_float(pml2[1]);
    const f4 o2 = __builtin_bit_cast(f4, po4);
    const float Ma = split ? M2 : M, La = split ? L2 : L, Mb = split ? M : M2, Lb = split ? L : L2;
    const float MM = fmaxf(Ma, Mb);
    const float wa = __builtin_amdgcn_exp2f(Ma - MM), wb = __builtin_amdgcn_exp2f(Mb - MM);
    const float inv = 1.f / fmaf(wb, Lb, wa * La);
    float r[4];
#pragma unroll
    for (int e = 0; e < 4; ++e) { const float oa = split ? o2[e] : o[e], ob = split ? o[e] : o2[e]; r[e] = fmaf(wb, ob, wa * oa) * inv; }
    v2u w; w[0] = pack_bf16(r[0], r[1]); w[1] = pack_bf16(r[2], r[3]);
    *reinterpret_cast<v2u*>(out + (size_t)row * (D / 2) + d0 / 2) = w;
    if (lane == 0) __hip_atomic_store(ctl, epoch + 2, __ATOMIC_RELAXED, __HIP_MEMORY_SCOPE_AGENT);
  }
}

}

extern "C" void kernel_launch(void* const* d_in, const int* in_sizes, int n_in, void* d_out, int out_size, void* d_ws, size_t ws_size,
                              hipStream_t stream) {
  const unsigned short* q = (const unsigned short*)d_in[0];
  const int* qk = (const int*)d_in[1];
  const int* qv = (const int*)d_in[2];
  const unsigned short* ks = (const unsigned short*)d_in[3];
  const unsigned short* kz = (const unsigned short*)d_in[4];
  const unsigned short* vs = (const unsigned short*)d_in[5];
  const unsigned short* vz = (const unsigned short*)d_in[6];
  attn_split<<<NB * HKV * NSPLIT, NTHR, 0, stream>>>(q, qk, qv, ks, kz, vs, vz, (unsigned char*)d_ws, (unsigned*)d_out
  );
}
```

```cpp
#include <hip/hip_runtime.h>
#include <hip/hip_bf16.h>
#include <stdint.h>

#define FUSED 1
#define MAPMODE 1
#define TLBWARM 1
#define UNROLL_ALL 0

namespace {
constexpr int NB = 8, SQ = 4, SKV = 2048, HQ = 32, HKV = 16, D = 128, NG = 16;
constexpr int MROWS = 8;
constexpr int WAVES = 4, NTHR = WAVES * 64;
constexpr int NSPLIT = 2;
constexpr int KEYS_WG = SKV / NSPLIT;
constexpr int TILES = KEYS_WG / (16 * WAVES);
constexpr int DEPTH = 4;
constexpr unsigned KV_BYTES = (unsigned)NB * HKV * SKV * D * 4u;
constexpr unsigned OOB = 0x80000000u;

typedef unsigned v4u __attribute__((ext_vector_type(4)));
typedef unsigned v2u __attribute__((ext_vector_type(2)));
typedef float f4 __attribute__((ext_vector_type(4)));
typedef float f2 __attribute__((ext_vector_type(2)));
typedef short s4v __attribute__((ext_vector_type(4)));
typedef __bf16 bf8v __attribute__((ext_vector_type(8)));
typedef __bf16 bf2v __attribute__((ext_vector_type(2)));
typedef unsigned long long u64;
typedef __attribute__((address_space(1))) u64 gu64;

__device__ __forceinline__ unsigned pack_bf16(float lo, float hi) {
  bf2v v; v[0] = (__bf16)lo; v[1] = (__bf16)hi;
  return __builtin_bit_cast(unsigned, v);
}
__device__ __forceinline__ float bf_lo(unsigned u) { return __builtin_bit_cast(float, u << 16); }
__device__ __forceinline__ float bf_hi(unsigned u) { return __builtin_bit_cast(float, u & 0xffff0000u); }
__device__ __forceinline__ float round_bf16(float x) { return bf_lo(pack_bf16(x, x)); }

__device__ __forceinline__ float xg_max(float x) {
  auto s = __builtin_amdgcn_permlane16_swap(__float_as_uint(x), __float_as_uint(x), false, false);
  x = fmaxf(__uint_as_float(s[0]), __uint_as_float(s[1]));
  auto t = __builtin_amdgcn_permlane32_swap(__float_as_uint(x), __float_as_uint(x), false, false);
  return fmaxf(__uint_as_float(t[0]), __uint_as_float(t[1]));
}
__device__ __forceinline__ float xg_sum(float x) {
  auto s = __builtin_amdgcn_permlane16_swap(__float_as_uint(x), __float_as_uint(x), false, false);
  x = __uint_as_float(s[0]) + __uint_as_float(s[1]);
  auto t = __builtin_amdgcn_permlane32_swap(__float_as_uint(x), __float_as_uint(x), false, false);
  return __uint_as_float(t[0]) + __uint_as_float(t[1]);
}
__device__ __forceinline__ void wave_sync() {
  __builtin_amdgcn_fence(__ATOMIC_RELEASE, "wavefront");
  __builtin_amdgcn_wave_barrier();
  __builtin_amdgcn_fence(__ATOMIC_ACQUIRE, "wavefront");
}
__device__ __forceinline__ u64 rfl64(u64 x) {
  const unsigned lo = __builtin_amdgcn_readfirstlane((unsigned)x), hi = __builtin_amdgcn_readfirstlane((unsigned)(x >> 32));
  return ((u64)hi << 32) | lo;
}

constexpr int LDS_T = 0;
constexpr int LDS_ML = 4096;
constexpr int LDS_S = 4096 + 64;
constexpr int LDS_WAVE = LDS_S + TILES * 1024;
constexpr int LDS_KS = WAVES * LDS_WAVE;
constexpr int LDS_VS = LDS_KS + 8192;
constexpr int LDS_BYTES = LDS_VS + 8192;

constexpr unsigned WS_BLK = 4096;

__global__ void __launch_bounds__(NTHR, 1)
attn_split(const unsigned short* __restrict__ q, const int* __restrict__ qk, const int* __restrict__ qv,
           const unsigned short* __restrict__ ks, const unsigned short* __restrict__ kz,
           const unsigned short* __restrict__ vs, const unsigned short* __restrict__ vz,
           unsigned char* __restrict__ ws, unsigned* __restrict__ out
           ) {
  __shared__ __attribute__((aligned(16))) unsigned char lds[LDS_BYTES];
  const int tid = threadIdx.x, lane = tid & 63, wave = __builtin_amdgcn_readfirstlane(tid >> 6);
  const int g = lane >> 4, c = lane & 15;
  const int bid = blockIdx.x;
  const int bh = ((bid >> 1) & 3) * 32 + (bid >> 3), split = bid & 1;
  const int key0 = split * KEYS_WG;

  v4u qf[4];
  {
    const unsigned short* qrow = q + ((size_t)bh * MROWS + (c & 7)) * D + 8 * g;
#pragma unroll
    for (int n = 0; n < 4; ++n) qf[n] = __builtin_nontemporal_load(reinterpret_cast<const v4u*>(qrow + 32 * n));
  }
  const size_t kso = ((size_t)bh * NG + split * (NG / NSPLIT)) * D + 4 * tid;
  const v2u ks_raw = __builtin_nontemporal_load(reinterpret_cast<const v2u*>(ks + kso));
  const v2u kz_raw = __builtin_nontemporal_load(reinterpret_cast<const v2u*>(kz + kso));
  const size_t vso = (size_t)bh * SKV + key0 + 4 * tid;
  const v2u vs_raw = __builtin_nontemporal_load(reinterpret_cast<const v2u*>(vs + vso));
  const v2u vz_raw = __builtin_nontemporal_load(reinterpret_cast<const v2u*>(vz + vso));
  gu64* ctl = (gu64*)(ws + (size_t)(bh * WAVES + wave) * WS_BLK);
  const u64 e_raw = __hip_atomic_load(ctl, __ATOMIC_RELAXED, __HIP_MEMORY_SCOPE_AGENT);

  const __amdgpu_buffer_rsrc_t rk = __builtin_amdgcn_make_buffer_rsrc((void*)qk, 0, KV_BYTES, 0x00020000);
  const __amdgpu_buffer_rsrc_t rv = __builtin_amdgcn_make_buffer_rsrc((void*)qv, 0, KV_BYTES, 0x00020000);
  const unsigned sbase = ((unsigned)bh * SKV + key0 + 16 * wave) * 512u;
  const unsigned voff = (unsigned)(4 * g) * 512u + (unsigned)c * 16u;
  unsigned warm0;
  {
    const __amdgpu_buffer_rsrc_t ro = __builtin_amdgcn_make_buffer_rsrc((void*)out, 0, NB * HQ * SQ * D * 2, 0x00020000);
    const __amdgpu_buffer_rsrc_t rwm = wave == 0 ? rv : (wave == 1 ? rk : ro);
    const unsigned wo = wave < 2 ? ((unsigned)bh * SKV + key0) * 512u : (unsigned)bh * (MROWS * D * 2);
    const unsigned lo = (lane < (wave < 2 ? 16 : 1)) ? (unsigned)lane * 32768u : OOB;
    warm0 = __builtin_amdgcn_raw_buffer_load_b32(rwm, lo, wo, 2);
  }
  v4u buf[DEPTH][8];
  auto issue = [&](const __amdgpu_buffer_rsrc_t& r, unsigned so, v4u (&dst)[8]) {
#pragma unroll
    for (int i = 0; i < 8; ++i) dst[i] = __builtin_amdgcn_raw_buffer_load_b128(r, voff + (i >> 1) * 512 + (i & 1) * 256, so, 2);
  };
  __builtin_amdgcn_sched_barrier(0);
#pragma unroll
  for (int d = 0; d < DEPTH; ++d) { issue(rk, sbase + d * (64 * 512), buf[d]); __builtin_amdgcn_sched_barrier(0); }

  {
    f4 s, n;
    s[0] = bf_lo(ks_raw[0]); s[1] = bf_hi(ks_raw[0]); s[2] = bf_lo(ks_raw[1]); s[3] = bf_hi(ks_raw[1]);
    n[0] = -bf_lo(kz_raw[0]) * s[0]; n[1] = -bf_hi(kz_raw[0]) * s[1]; n[2] = -bf_lo(kz_raw[1]) * s[2]; n[3] = -bf_hi(kz_raw[1]) * s[3];
    *reinterpret_cast<f4*>(lds + LDS_KS + tid * 32) = s;
    *reinterpret_cast<f4*>(lds + LDS_KS + tid * 32 + 16) = n;
    s[0] = bf_lo(vs_raw[0]); s[1] = bf_hi(vs_raw[0]); s[2] = bf_lo(vs_raw[1]); s[3] = bf_hi(vs_raw[1]);
    n[0] = -bf_lo(vz_raw[0]) * s[0]; n[1] = -bf_hi(vz_raw[0]) * s[1]; n[2] = -bf_lo(vz_raw[1]) * s[2]; n[3] = -bf_hi(vz_raw[1]) * s[3];
    *reinterpret_cast<f4*>(lds + LDS_VS + tid * 32) = s;
    *reinterpret_cast<f4*>(lds + LDS_VS + tid * 32 + 16) = n;
  }
  const u64 epoch = rfl64(e_raw);
  __syncthreads();

  unsigned char* wl = lds + wave * LDS_WAVE;
  constexpr float SCALE = 0.08838834764831845f;
  constexpr float LOG2E = 1.4426950408889634f;
  float m = -INFINITY;
#pragma unroll 1
  for (int t4 = 0; t4 < TILES; t4 += DEPTH) {
    const bool last = t4 + DEPTH >= TILES;
    const __amdgpu_buffer_rsrc_t rn = last ? rv : rk;
    const unsigned sonext = sbase + (last ? 0u : (unsigned)(t4 + DEPTH) * (64 * 512));
#pragma unroll
    for (int s = 0; s < DEPTH; ++s) {
      const int t = t4 + s;
      v4u (&x)[8] = buf[s];
      const unsigned char* kls = lds + LDS_KS + ((t >> 1) * 32 + c) * 32;
      f4 sc[2], nz[2];
#pragma unroll
      for (int h = 0; h < 2; ++h) { sc[h] = *reinterpret_cast<const f4*>(kls + h * 512); nz[h] = *reinterpret_cast<const f4*>(kls + h * 512 + 16); }
#pragma unroll
      for (int i = 0; i < 8; ++i) {
        const int j = i >> 1, h = i & 1;
        const int r = 4 * g + j;
        float f[4];
#pragma unroll
        for (int e = 0; e < 4; ++e) f[e] = fmaf((float)(int)x[i][e], sc[h][e], nz[h][e]);
        v2u w; w[0] = pack_bf16(f[0], f[1]); w[1] = pack_bf16(f[2], f[3]);
        const int chunk = (8 * h + (c >> 1)) ^ r;
        *reinterpret_cast<v2u*>(wl + LDS_T + r * 256 + chunk * 16 + (c & 1) * 8) = w;
      }
      __builtin_amdgcn_sched_barrier(0);
      issue(rn, sonext + s * (64 * 512), buf[s]);
      __builtin_amdgcn_sched_barrier(0);
      wave_sync();
      f4 acc = {0.f, 0.f, 0.f, 0.f};
#pragma unroll
      for (int n = 0; n < 4; ++n) {
        const v4u a = *reinterpret_cast<const v4u*>(wl + LDS_T + c * 256 + (((4 * n + g) ^ c) * 16));
        acc = __builtin_amdgcn_mfma_f32_16x16x32_bf16(__builtin_bit_cast(bf8v, a), __builtin_bit_cast(bf8v, qf[n]), acc, 0, 0, 0);
      }
#pragma unroll
      for (int r = 0; r < 4; ++r) { acc[r] = round_bf16(round_bf16(acc[r]) * SCALE) * LOG2E; m = fmaxf(m, acc[r]); }
      *reinterpret_cast<f4*>(wl + LDS_S + t * 1024 + lane * 16) = acc;
      wave_sync();
    }
  }
  asm volatile("" :: "v"(warm0));
  m = xg_max(m);
  float l = 0.f;
  f4 O[8];
#pragma unroll
  for (int k = 0; k < 8; ++k) O[k] = f4{0.f, 0.f, 0.f, 0.f};
  auto vtile = [&](int t, v4u (&x)[8], bool refill) {
      const f4 sv = *reinterpret_cast<const f4*>(wl + LDS_S + t * 1024 + lane * 16);
      const unsigned char* vls = lds + LDS_VS + (t * 16 + 4 * wave + g) * 32;
      const f4 sc = *reinterpret_cast<const f4*>(vls), nz = *reinterpret_cast<const f4*>(vls + 16);
      float p[4];
#pragma unroll
      for (int r = 0; r < 4; ++r) { p[r] = __builtin_amdgcn_exp2f(sv[r] - m); l += p[r]; }
      v2u P; P[0] = pack_bf16(p[0], p[1]); P[1] = pack_bf16(p[2], p[3]);
      v2u a[8];
#pragma unroll
      for (int h = 0; h < 2; ++h)
#pragma unroll
        for (int e = 0; e < 4; ++e) {
          float f[4];
#pragma unroll
          for (int j = 0; j < 4; ++j) f[j] = fmaf((float)(int)x[2 * j + h][e], sc[j], nz[j]);
          a[h * 4 + e][0] = pack_bf16(f[0], f[1]); a[h * 4 + e][1] = pack_bf16(f[2], f[3]);
        }
      if (refill) {
        asm volatile("" : "+v"(a[0]), "+v"(a[1]), "+v"(a[2]), "+v"(a[3]), "+v"(a[4]), "+v"(a[5]), "+v"(a[6]), "+v"(a[7]) : : "memory");
        __builtin_amdgcn_sched_barrier(0);
        issue(rv, sbase + (unsigned)(t + DEPTH) * (64 * 512), x);
        __builtin_amdgcn_sched_barrier(0);
      }
#pragma unroll
      for (int k = 0; k < 8; ++k)
        O[k] = __builtin_amdgcn_mfma_f32_16x16x16bf16_1k(__builtin_bit_cast(s4v, a[k]), __builtin_bit_cast(s4v, P), O[k], 0, 0, 0);
  };
#pragma unroll 1
  for (int t4 = 0; t4 < TILES - DEPTH; t4 += DEPTH) {
#pragma unroll
    for (int s = 0; s < DEPTH; ++s) vtile(t4 + s, buf[s], true);
  }
  const u64 peek_raw = __hip_atomic_load(ctl + 1, __ATOMIC_RELAXED, __HIP_MEMORY_SCOPE_AGENT);
  __builtin_amdgcn_sched_barrier(0);
#pragma unroll
  for (int s = 0; s < DEPTH; ++s) { vtile(TILES - DEPTH + s, buf[s], false); __builtin_amdgcn_sched_barrier(0); }
  l = xg_sum(l);
  const int qr = 2 * wave + (lane >> 5), d0 = 4 * (lane & 31);
  const unsigned row = (unsigned)bh * MROWS + qr;
  const __amdgpu_buffer_rsrc_t rw = __builtin_amdgcn_make_buffer_rsrc((void*)ws, 0, 4u << 20, 0x00020000);
  const unsigned blk = (unsigned)(bh * WAVES + wave) * WS_BLK;
  const unsigned po_me = blk + 1024u + split * 1024u + lane * 16u, po_ot = blk + 1024u + (split ^ 1) * 1024u + lane * 16u;
  const unsigned ml_me = blk + 128u + split * 128u + (lane >> 5) * 8u, ml_ot = blk + 128u + (split ^ 1) * 128u + (lane >> 5) * 8u;
  bool have = rfl64(peek_raw) == epoch + 2;
  v4u po4 = __builtin_amdgcn_raw_buffer_load_b128(rw, po_ot, have ? 0u : OOB, 16);
  v2u pml2 = __builtin_amdgcn_raw_buffer_load_b64(rw, ml_ot, have ? 0u : OOB, 16);
  __builtin_amdgcn_sched_barrier(0);
  {
    float* ow = reinterpret_cast<float*>(wl + LDS_T) + (c & 7) * 128 + (c >> 3) * 64 + 16 * g;
#pragma unroll
    for (int r = 0; r < 4; ++r) {
      f4 v;
#pragma unroll
      for (int e = 0; e < 4; ++e) v[e] = (c < 8) ? O[e][r] : O[4 + e][r];
      *reinterpret_cast<f4*>(ow + 4 * r) = v;
    }
    if (lane < 8) *reinterpret_cast<f2*>(wl + LDS_ML + lane * 8) = f2{m, l};
  }
  __syncthreads();
  float M = -INFINITY, L = 0.f;
  f4 o = {0.f, 0.f, 0.f, 0.f};
  {
    f2 ml[WAVES];
#pragma unroll
    for (int w = 0; w < WAVES; ++w) { ml[w] = *reinterpret_cast<const f2*>(lds + w * LDS_WAVE + LDS_ML + qr * 8); M = fmaxf(M, ml[w][0]); }
#pragma unroll
    for (int w = 0; w < WAVES; ++w) {
      const float wt = __builtin_amdgcn_exp2f(ml[w][0] - M);
      const f4 ov = *reinterpret_cast<const f4*>(lds + w * LDS_WAVE + LDS_T + (qr * 128 + d0) * 4);
      L = fmaf(wt, ml[w][1], L);
#pragma unroll
      for (int e = 0; e < 4; ++e) o[e] = fmaf(wt, ov[e], o[e]);
    }
  }
  bool fin = true;
  if (!have) {
    __builtin_amdgcn_raw_buffer_store_b128(__builtin_bit_cast(v4u, o), rw, po_me, 0, 16);
    __builtin_amdgcn_raw_buffer_store_b64(__builtin_bit_cast(v2u, f2{M, L}), rw, (lane & 31) == 0 ? ml_me : OOB, 0, 16);
    u64 old = 0;
    if (lane == 0) old = __hip_atomic_exchange(ctl + 1, epoch + 1, __ATOMIC_RELAXED, __HIP_MEMORY_SCOPE_AGENT);
    asm volatile("s_waitcnt vmcnt(0)" ::: "memory");
    old = rfl64(old);
    if (old == epoch + 1 || old == epoch + 2) {
      while (old != epoch + 2) {
        __builtin_amdgcn_s_sleep(2);
        old = rfl64(__hip_atomic_load(ctl + 1, __ATOMIC_RELAXED, __HIP_MEMORY_SCOPE_AGENT));
      }
      asm volatile("" ::: "memory");
      po4 = __builtin_amdgcn_raw_buffer_load_b128(rw, po_ot, 0, 16);
      pml2 = __builtin_amdgcn_raw_buffer_load_b64(rw, ml_ot, 0, 16);
    } else {
      if (lane == 0) __hip_atomic_store(ctl + 1, epoch + 2, __ATOMIC_RELAXED, __HIP_MEMORY_SCOPE_AGENT);
      fin = false;
    }
  }
  if (fin) {
    const float M2 = __uint_as_float(pml2[0]), L2 = __uint_as_float(pml2[1]);
    const f4 o2 = __builtin_bit_cast(f4, po4);
    const float Ma = split ? M2 : M, La = split ? L2 : L, Mb = split ? M : M2, Lb = split ? L : L2;
    const float MM = fmaxf(Ma, Mb);
    const float wa = __builtin_amdgcn_exp2f(Ma - MM), wb = __builtin_amdgcn_exp2f(Mb - MM);
    const float inv = 1.f / fmaf(wb, Lb, wa * La);
    float r[4];
#pragma unroll
    for (int e = 0; e < 4; ++e) { const float oa = split ? o2[e] : o[e], ob = split ? o[e] : o2[e]; r[e] = fmaf(wb, ob, wa * oa) * inv; }
    v2u w; w[0] = pack_bf16(r[0], r[1]); w[1] = pack_bf16(r[2], r[3]);
    *reinterpret_cast<v2u*>(out + (size_t)row * (D / 2) + d0 / 2) = w;
    if (lane == 0) __hip_atomic_store(ctl, epoch + 2, __ATOMIC_RELAXED, __HIP_MEMORY_SCOPE_AGENT);
  }
}

}

extern "C" void kernel_launch(void* const* d_in, const int* in_sizes, int n_in, void* d_out, int out_size, void* d_ws, size_t ws_size,
                              hipStream_t stream) {
  const unsigned short* q = (const unsigned short*)d_in[0];
  const int* qk = (const int*)d_in[1];
  const int* qv = (const int*)d_in[2];
  const unsigned short* ks = (const unsigned short*)d_in[3];
  const unsigned short* kz = (const unsigned short*)d_in[4];
  const unsigned short* vs = (const unsigned short*)d_in[5];
  const unsigned short* vz = (const unsigned short*)d_in[6];
  attn_split<<<NB * HKV * NSPLIT, NTHR, 0, stream>>>(q, qk, qv, ks, kz, vs, vz, (unsigned char*)d_ws, (unsigned*)d_out
  );
}
```

```cpp
#include <hip/hip_runtime.h>
#include <hip/hip_bf16.h>
#include <stdint.h>

#define WIN 6
#define MAPMODE 1
#define ROT 0

namespace {
constexpr int NB = 8, SQ = 4, SKV = 2048, HQ = 32, HKV = 16, D = 128, NG = 16;
constexpr int MROWS = 8;
constexpr int WAVES = 4, NTHR = WAVES * 64;
constexpr int NSPLIT = 2;
constexpr int KEYS_WG = SKV / NSPLIT;
constexpr int TILES = KEYS_WG / (16 * WAVES);
constexpr unsigned KV_BYTES = (unsigned)NB * HKV * SKV * D * 4u;
constexpr unsigned OOB = 0x80000000u;
constexpr unsigned TSTRIDE = 64 * 512;
static_assert(WIN >= 1 && WIN <= 8, "window");

typedef unsigned v4u __attribute__((ext_vector_type(4)));
typedef unsigned v2u __attribute__((ext_vector_type(2)));
typedef float f4 __attribute__((ext_vector_type(4)));
typedef float f2 __attribute__((ext_vector_type(2)));
typedef short s4v __attribute__((ext_vector_type(4)));
typedef __bf16 bf8v __attribute__((ext_vector_type(8)));
typedef __bf16 bf2v __attribute__((ext_vector_type(2)));
typedef unsigned long long u64;
typedef __attribute__((address_space(1))) u64 gu64;

__device__ __forceinline__ unsigned pack_bf16(float lo, float hi) {
  bf2v v; v[0] = (__bf16)lo; v[1] = (__bf16)hi;
  return __builtin_bit_cast(unsigned, v);
}
__device__ __forceinline__ float bf_lo(unsigned u) { return __uint_as_float(u << 16); }
__device__ __forceinline__ float bf_hi(unsigned u) { return __uint_as_float(u & 0xffff0000u); }
__device__ __forceinline__ float round_bf16(float x) { return bf_lo(pack_bf16(x, x)); }

__device__ __forceinline__ float xg_max(float x) {
  auto s = __builtin_amdgcn_permlane16_swap(__float_as_uint(x), __float_as_uint(x), false, false);
  x = fmaxf(__uint_as_float(s[0]), __uint_as_float(s[1]));
  auto t = __builtin_amdgcn_permlane32_swap(__float_as_uint(x), __float_as_uint(x), false, false);
  return fmaxf(__uint_as_float(t[0]), __uint_as_float(t[1]));
}
__device__ __forceinline__ float xg_sum(float x) {
  auto s = __builtin_amdgcn_permlane16_swap(__float_as_uint(x), __float_as_uint(x), false, false);
  x = __uint_as_float(s[0]) + __uint_as_float(s[1]);
  auto t = __builtin_amdgcn_permlane32_swap(__float_as_uint(x), __float_as_uint(x), false, false);
  return __uint_as_float(t[0]) + __uint_as_float(t[1]);
}
__device__ __forceinline__ void wave_sync() {
  __builtin_amdgcn_fence(__ATOMIC_RELEASE, "wavefront");
  __builtin_amdgcn_wave_barrier();
  __builtin_amdgcn_fence(__ATOMIC_ACQUIRE, "wavefront");
}
__device__ __forceinline__ u64 rfl64(u64 x) {
  const unsigned lo = __builtin_amdgcn_readfirstlane((unsigned)x), hi = __builtin_amdgcn_readfirstlane((unsigned)(x >> 32));
  return ((u64)hi << 32) | lo;
}
#define SB() __builtin_amdgcn_sched_barrier(0)

constexpr int LDS_T = 0;
constexpr int LDS_ML = 4096;
constexpr int LDS_S = 4096 + 64;
constexpr int LDS_WAVE = LDS_S + TILES * 1024;
constexpr int LDS_KS = WAVES * LDS_WAVE;
constexpr int LDS_VS = LDS_KS + 8192;
constexpr int LDS_BYTES = LDS_VS + 8192;

constexpr unsigned WS_BLK = 4096;

__global__ void __launch_bounds__(NTHR, 1)
attn_split(const unsigned short* __restrict__ q, const int* __restrict__ qk, const int* __restrict__ qv,
           const unsigned short* __restrict__ ks, const unsigned short* __restrict__ kz,
           const unsigned short* __restrict__ vs, const unsigned short* __restrict__ vz,
           unsigned char* __restrict__ ws, unsigned* __restrict__ out
           ) {
  __shared__ __attribute__((aligned(16))) unsigned char lds[LDS_BYTES];
  const int tid = threadIdx.x, lane = tid & 63, wave = __builtin_amdgcn_readfirstlane(tid >> 6);
  const int g = lane >> 4, c = lane & 15;
  const int bid = blockIdx.x;
  const int bh = ((bid >> 1) & 3) * 32 + (bid >> 3), split = bid & 1;
  const int key0 = split * KEYS_WG;
  const int rot = (bh * ROT) & (TILES - 1);

  const __amdgpu_buffer_rsrc_t rk = __builtin_amdgcn_make_buffer_rsrc((void*)qk, 0, KV_BYTES, 0x00020000);
  const __amdgpu_buffer_rsrc_t rv = __builtin_amdgcn_make_buffer_rsrc((void*)qv, 0, KV_BYTES, 0x00020000);
  const unsigned sbase = ((unsigned)bh * SKV + key0 + 16 * wave) * 512u;
  const unsigned voff = (unsigned)(4 * g) * 512u + (unsigned)c * 16u;
  v4u buf[8];
#define LOADKV(r, i, so) __builtin_amdgcn_raw_buffer_load_b128((r), voff + (i) * 256, (so), 2)

  v4u qf[4];
  {
    const unsigned short* qrow = q + ((size_t)bh * MROWS + (c & 7)) * D + 8 * g;
#pragma unroll
    for (int n = 0; n < 4; ++n) qf[n] = __builtin_nontemporal_load(reinterpret_cast<const v4u*>(qrow + 32 * n));
  }
  const size_t kso = ((size_t)bh * NG + split * (NG / NSPLIT)) * D + 4 * tid;
  const v2u ks_raw = __builtin_nontemporal_load(reinterpret_cast<const v2u*>(ks + kso));
  const v2u kz_raw = __builtin_nontemporal_load(reinterpret_cast<const v2u*>(kz + kso));
  const size_t vso = (size_t)bh * SKV + key0 + 4 * tid;
  const v2u vs_raw = __builtin_nontemporal_load(reinterpret_cast<const v2u*>(vs + vso));
  const v2u vz_raw = __builtin_nontemporal_load(reinterpret_cast<const v2u*>(vz + vso));
  gu64* ctl = (gu64*)(ws + (size_t)(bh * WAVES + wave) * WS_BLK);
  const u64 e_raw = __hip_atomic_load(ctl, __ATOMIC_RELAXED, __HIP_MEMORY_SCOPE_AGENT);
  SB();
  {
    const unsigned so0 = sbase + (unsigned)rot * TSTRIDE;
#pragma unroll
    for (int i = 0; i < WIN; ++i) { buf[i] = LOADKV(rk, i, so0); SB(); }
  }

  {
    f4 s, n;
    s[0] = bf_lo(ks_raw[0]); s[1] = bf_hi(ks_raw[0]); s[2] = bf_lo(ks_raw[1]); s[3] = bf_hi(ks_raw[1]);
    n[0] = -bf_lo(kz_raw[0]) * s[0]; n[1] = -bf_hi(kz_raw[0]) * s[1]; n[2] = -bf_lo(kz_raw[1]) * s[2]; n[3] = -bf_hi(kz_raw[1]) * s[3];
    *reinterpret_cast<f4*>(lds + LDS_KS + tid * 32) = s;
    *reinterpret_cast<f4*>(lds + LDS_KS + tid * 32 + 16) = n;
    s[0] = bf_lo(vs_raw[0]); s[1] = bf_hi(vs_raw[0]); s[2] = bf_lo(vs_raw[1]); s[3] = bf_hi(vs_raw[1]);
    n[0] = -bf_lo(vz_raw[0]) * s[0]; n[1] = -bf_hi(vz_raw[0]) * s[1]; n[2] = -bf_lo(vz_raw[1]) * s[2]; n[3] = -bf_hi(vz_raw[1]) * s[3];
    *reinterpret_cast<f4*>(lds + LDS_VS + tid * 32) = s;
    *reinterpret_cast<f4*>(lds + LDS_VS + tid * 32 + 16) = n;
  }
  const u64 epoch = rfl64(e_raw);
  __syncthreads();

  unsigned char* wl = lds + wave * LDS_WAVE;
  constexpr float SCALE = 0.08838834764831845f;
  constexpr float LOG2E = 1.4426950408889634f;
  float m = -INFINITY;
#pragma unroll 1
  for (int s = 0; s < TILES; ++s) {
    const int t = (s + rot) & (TILES - 1), tn = (s + 1 + rot) & (TILES - 1);
    const bool last = s == TILES - 1;
    const __amdgpu_buffer_rsrc_t rn = last ? rv : rk;
    const unsigned so = sbase + (unsigned)t * TSTRIDE, son = sbase + (unsigned)tn * TSTRIDE;
    const unsigned char* kls = lds + LDS_KS + ((t >> 1) * 32 + c) * 32;
    f4 sc[2], nz[2];
#pragma unroll
    for (int h = 0; h < 2; ++h) { sc[h] = *reinterpret_cast<const f4*>(kls + h * 512); nz[h] = *reinterpret_cast<const f4*>(kls + h * 512 + 16); }
#pragma unroll
    for (int i = 0; i < 8; ++i) {
      const int j = i >> 1, h = i & 1;
      const int r = 4 * g + j;
      float f[4];
#pragma unroll
      for (int e = 0; e < 4; ++e) f[e] = fmaf((float)(int)buf[i][e], sc[h][e], nz[h][e]);
      v2u w; w[0] = pack_bf16(f[0], f[1]); w[1] = pack_bf16(f[2], f[3]);
      const int chunk = (8 * h + (c >> 1)) ^ r;
      *reinterpret_cast<v2u*>(wl + LDS_T + r * 256 + chunk * 16 + (c & 1) * 8) = w;
      SB();
      if (i + WIN < 8) buf[i + WIN] = LOADKV(rk, i + WIN, so); else buf[i + WIN - 8] = LOADKV(rn, i + WIN - 8, son);
      SB();
    }
    wave_sync();
    f4 acc = {0.f, 0.f, 0.f, 0.f};
#pragma unroll
    for (int n = 0; n < 4; ++n) {
      const v4u a = *reinterpret_cast<const v4u*>(wl + LDS_T + c * 256 + (((4 * n + g) ^ c) * 16));
      acc = __builtin_amdgcn_mfma_f32_16x16x32_bf16(__builtin_bit_cast(bf8v, a), __builtin_bit_cast(bf8v, qf[n]), acc, 0, 0, 0);
    }
#pragma unroll
    for (int r = 0; r < 4; ++r) { acc[r] = round_bf16(round_bf16(acc[r]) * SCALE) * LOG2E; m = fmaxf(m, acc[r]); }
    *reinterpret_cast<f4*>(wl + LDS_S + s * 1024 + lane * 16) = acc;
    wave_sync();
  }
  m = xg_max(m);
  float l = 0.f;
  f4 O[8];
#pragma unroll
  for (int k = 0; k < 8; ++k) O[k] = f4{0.f, 0.f, 0.f, 0.f};
  u64 peek_raw = 0;
  auto vtile = [&](int s, bool lastv) {
    const int t = (s + rot) & (TILES - 1), tn = (s + 1 + rot) & (TILES - 1);
    const unsigned so = sbase + (unsigned)t * TSTRIDE, son = sbase + (unsigned)tn * TSTRIDE;
    const f4 sv = *reinterpret_cast<const f4*>(wl + LDS_S + s * 1024 + lane * 16);
    const unsigned char* vls = lds + LDS_VS + (t * 16 + 4 * wave + g) * 32;
    const f4 sc = *reinterpret_cast<const f4*>(vls), nz = *reinterpret_cast<const f4*>(vls + 16);
    float p[4];
#pragma unroll
    for (int r = 0; r < 4; ++r) { p[r] = __builtin_amdgcn_exp2f(sv[r] - m); l += p[r]; }
    v2u P; P[0] = pack_bf16(p[0], p[1]); P[1] = pack_bf16(p[2], p[3]);
    f4 fe[2];
    v2u a[8];
#pragma unroll
    for (int i = 0; i < 8; ++i) {
      const int j = i >> 1, h = i & 1;
      f4 f;
#pragma unroll
      for (int e = 0; e < 4; ++e) f[e] = fmaf((float)(int)buf[i][e], sc[j], nz[j]);
      if ((j & 1) == 0) { fe[h] = f; asm volatile("" : "+v"(fe[h])); }
      else {
#pragma unroll
        for (int e = 0; e < 4; ++e) { unsigned pk = pack_bf16(fe[h][e], f[e]); asm volatile("" : "+v"(pk)); a[h * 4 + e][j >> 1] = pk; }
      }
      SB();
      if (i + WIN < 8) buf[i + WIN] = LOADKV(rv, i + WIN, so);
      else if (!lastv) buf[i + WIN - 8] = LOADKV(rv, i + WIN - 8, son);
      else if (i + WIN == 8) peek_raw = __hip_atomic_load(ctl + 1, __ATOMIC_RELAXED, __HIP_MEMORY_SCOPE_AGENT);
      SB();
      if (j == 3) {
#pragma unroll
        for (int e = 0; e < 4; ++e)
          O[h * 4 + e] = __builtin_amdgcn_mfma_f32_16x16x16bf16_1k(__builtin_bit_cast(s4v, a[h * 4 + e]), __builtin_bit_cast(s4v, P), O[h * 4 + e], 0, 0, 0);
      }
    }
  };
#pragma unroll 1
  for (int s = 0; s < TILES - 1; ++s) vtile(s, false);
  vtile(TILES - 1, true);
  l = xg_sum(l);
  const int qr = 2 * wave + (lane >> 5), d0 = 4 * (lane & 31);
  const unsigned row = (unsigned)bh * MROWS + qr;
  const __amdgpu_buffer_rsrc_t rw = __builtin_amdgcn_make_buffer_rsrc((void*)ws, 0, 4u << 20, 0x00020000);
  const unsigned blk = (unsigned)(bh * WAVES + wave) * WS_BLK;
  const unsigned po_me = blk + 1024u + split * 1024u + lane * 16u, po_ot = blk + 1024u + (split ^ 1) * 1024u + lane * 16u;
  const unsigned ml_me = blk + 128u + split * 128u + (lane >> 5) * 8u, ml_ot = blk + 128u + (split ^ 1) * 128u + (lane >> 5) * 8u;
  const bool have = rfl64(peek_raw) == epoch + 2;
  v4u po4 = __builtin_amdgcn_raw_buffer_load_b128(rw, po_ot, have ? 0u : OOB, 16);
  v2u pml2 = __builtin_amdgcn_raw_buffer_load_b64(rw, ml_ot, have ? 0u : OOB, 16);
  SB();
  {
    float* ow = reinterpret_cast<float*>(wl + LDS_T) + (c & 7) * 128 + (c >> 3) * 64 + 16 * g;
#pragma unroll
    for (int r = 0; r < 4; ++r) {
      f4 v;
#pragma unroll
      for (int e = 0; e < 4; ++e) v[e] = (c < 8) ? O[e][r] : O[4 + e][r];
      *reinterpret_cast<f4*>(ow + 4 * r) = v;
    }
    if (lane < 8) *reinterpret_cast<f2*>(wl + LDS_ML + lane * 8) = f2{m, l};
  }
  __syncthreads();
  float M = -INFINITY, L = 0.f;
  f4 o = {0.f, 0.f, 0.f, 0.f};
  {
    f2 ml[WAVES];
#pragma unroll
    for (int w = 0; w < WAVES; ++w) { ml[w] = *reinterpret_cast<const f2*>(lds + w * LDS_WAVE + LDS_ML + qr * 8); M = fmaxf(M, ml[w][0]); }
#pragma unroll
    for (int w = 0; w < WAVES; ++w) {
      const float wt = __builtin_amdgcn_exp2f(ml[w][0] - M);
      const f4 ov = *reinterpret_cast<const f4*>(lds + w * LDS_WAVE + LDS_T + (qr * 128 + d0) * 4);
      L = fmaf(wt, ml[w][1], L);
#pragma unroll
      for (int e = 0; e < 4; ++e) o[e] = fmaf(wt, ov[e], o[e]);
    }
  }
  bool fin = true;
  if (!have) {
    __builtin_amdgcn_raw_buffer_store_b128(__builtin_bit_cast(v4u, o), rw, po_me, 0, 16);
    __builtin_amdgcn_raw_buffer_store_b64(__builtin_bit_cast(v2u, f2{M, L}), rw, (lane & 31) == 0 ? ml_me : OOB, 0, 16);
    u64 old = 0;
    if (lane == 0) old = __hip_atomic_exchange(ctl + 1, epoch + 1, __ATOMIC_RELAXED, __HIP_MEMORY_SCOPE_AGENT);
    asm volatile("s_waitcnt vmcnt(0)" ::: "memory");
    old = rfl64(old);
    if (old == epoch + 1 || old == epoch + 2) {
      while (old != epoch + 2) {
        __builtin_amdgcn_s_sleep(2);
        old = rfl64(__hip_atomic_load(ctl + 1, __ATOMIC_RELAXED, __HIP_MEMORY_SCOPE_AGENT));
      }
      asm volatile("" ::: "memory");
      po4 = __builtin_amdgcn_raw_buffer_load_b128(rw, po_ot, 0, 16);
      pml2 = __builtin_amdgcn_raw_buffer_load_b64(rw, ml_ot, 0, 16);
    } else {
      if (lane == 0) __hip_atomic_store(ctl + 1, epoch + 2, __ATOMIC_RELAXED, __HIP_MEMORY_SCOPE_AGENT);
      fin = false;
    }
  }
  if (fin) {
    const float M2 = __uint_as_float(pml2[0]), L2 = __uint_as_float(pml2[1]);
    const f4 o2 = __builtin_bit_cast(f4, po4);
    const float Ma = split ? M2 : M, La = split ? L2 : L, Mb = split ? M : M2, Lb = split ? L : L2;
    const float MM = fmaxf(Ma, Mb);
    const float wa = __builtin_amdgcn_exp2f(Ma - MM), wb = __builtin_amdgcn_exp2f(Mb - MM);
    const float inv = 1.f / fmaf(wb, Lb, wa * La);
    float r[4];
#pragma unroll
    for (int e = 0; e < 4; ++e) { const float oa = split ? o2[e] : o[e], ob = split ? o[e] : o2[e]; r[e] = fmaf(wb, ob, wa * oa) * inv; }
    v2u w; w[0] = pack_bf16(r[0], r[1]); w[1] = pack_bf16(r[2], r[3]);
    *reinterpret_cast<v2u*>(out + (size_t)row * (D / 2) + d0 / 2) = w;
    if (lane == 0) __hip_atomic_store(ctl, epoch + 2, __ATOMIC_RELAXED, __HIP_MEMORY_SCOPE_AGENT);
  }
}
}

extern "C" void kernel_launch(void* const* d_in, const int* in_sizes, int n_in, void* d_out, int out_size, void* d_ws, size_t ws_size,
                              hipStream_t stream) {
  const unsigned short* q = (const unsigned short*)d_in[0];
  const int* qk = (const int*)d_in[1];
  const int* qv = (const int*)d_in[2];
  const unsigned short* ks = (const unsigned short*)d_in[3];
  const unsigned short* kz = (const unsigned short*)d_in[4];
  const unsigned short* vs = (const unsigned short*)d_in[5];
  const unsigned short* vz = (const unsigned short*)d_in[6];
  attn_split<<<NB * HKV * NSPLIT, NTHR, 0, stream>>>(q, qk, qv, ks, kz, vs, vz, (unsigned char*)d_ws, (unsigned*)d_out
  );
}
```

```cpp
#include <hip/hip_runtime.h>
#include <hip/hip_bf16.h>
#include <stdint.h>

namespace {
constexpr int NB = 8, SQ = 4, SKV = 2048, HQ = 32, HKV = 16, D = 128, NG = 16;
constexpr int MROWS = (HQ / HKV) * SQ;
constexpr int WAVES = 4, NTHR = WAVES * 64;
constexpr int NSPLIT = 2;
constexpr int KEYS_WG = SKV / NSPLIT;
constexpr int TILES = KEYS_WG / (16 * WAVES);
constexpr unsigned KV_BYTES = (unsigned)NB * HKV * SKV * D * 4u;
constexpr unsigned OOB = 0x80000000u;
constexpr unsigned TSTRIDE = WAVES * 16 * 512;
static_assert(MROWS == 8 && TILES == 16 && SKV / NG == 128, "layout below is written for these shapes");

constexpr int TAU_EARLY16 = 1616, TAU_LATE16 = 1864;
constexpr int PACE_OFF = 220, PACE_BAND = 30, PACE_ANCHOR = 230;
constexpr int LVL_EARLY = 2, LVL_LATE = 3;
constexpr int SPRINT_LATE = 26;
constexpr int KFIRST = 4;
constexpr int PEEK_STEP = 4, FETCH_STEP = 15;

typedef unsigned v4u __attribute__((ext_vector_type(4)));
typedef unsigned v2u __attribute__((ext_vector_type(2)));
typedef float f4 __attribute__((ext_vector_type(4)));
typedef float f2 __attribute__((ext_vector_type(2)));
typedef short s4v __attribute__((ext_vector_type(4)));
typedef __bf16 bf8v __attribute__((ext_vector_type(8)));
typedef __bf16 bf2v __attribute__((ext_vector_type(2)));
typedef unsigned long long u64;
typedef __attribute__((address_space(1))) u64 gu64;

__device__ __forceinline__ unsigned pack_bf16(float lo, float hi) {
  bf2v v; v[0] = (__bf16)lo; v[1] = (__bf16)hi;
  return __builtin_bit_cast(unsigned, v);
}
__device__ __forceinline__ float bf_lo(unsigned u) { return __uint_as_float(u << 16); }
__device__ __forceinline__ float bf_hi(unsigned u) { return __uint_as_float(u & 0xffff0000u); }
__device__ __forceinline__ float round_bf16(float x) { return bf_lo(pack_bf16(x, x)); }

__device__ __forceinline__ float xg_max(float x) {
  auto s = __builtin_amdgcn_permlane16_swap(__float_as_uint(x), __float_as_uint(x), false, false);
  x = fmaxf(__uint_as_float(s[0]), __uint_as_float(s[1]));
  auto t = __builtin_amdgcn_permlane32_swap(__float_as_uint(x), __float_as_uint(x), false, false);
  return fmaxf(__uint_as_float(t[0]), __uint_as_float(t[1]));
}
__device__ __forceinline__ float xg_sum(float x) {
  auto s = __builtin_amdgcn_permlane16_swap(__float_as_uint(x), __float_as_uint(x), false, false);
  x = __uint_as_float(s[0]) + __uint_as_float(s[1]);
  auto t = __builtin_amdgcn_permlane32_swap(__float_as_uint(x), __float_as_uint(x), false, false);
  return __uint_as_float(t[0]) + __uint_as_float(t[1]);
}
__device__ __forceinline__ void wave_sync() {
  __builtin_amdgcn_fence(__ATOMIC_RELEASE, "wavefront");
  __builtin_amdgcn_wave_barrier();
  __builtin_amdgcn_fence(__ATOMIC_ACQUIRE, "wavefront");
}
__device__ __forceinline__ u64 rfl64(u64 x) {
  const unsigned lo = __builtin_amdgcn_readfirstlane((unsigned)x), hi = __builtin_amdgcn_readfirstlane((unsigned)(x >> 32));
  return ((u64)hi << 32) | lo;
}
__device__ __forceinline__ u64 mix64(u64 z) {
  z = (z ^ (z >> 30)) * 0xbf58476d1ce4e5b9ull;
  z = (z ^ (z >> 27)) * 0x94d049bb133111ebull;
  return z ^ (z >> 31);
}
#define SB() __builtin_amdgcn_sched_barrier(0)
__device__ __forceinline__ void throttle(int lvl) {
  asm volatile("s_cmp_lt_u32 %0, 3\n\ts_cbranch_scc1 2f\n\t"
               "s_waitcnt vmcnt(5)\n\ts_cmp_eq_u32 %0, 3\n\ts_cbranch_scc1 1f\n\ts_waitcnt vmcnt(4)\n\ts_branch 1f\n"
               "2:\n\ts_cmp_eq_u32 %0, 0\n\ts_cbranch_scc1 1f\n\ts_waitcnt vmcnt(7)\n\ts_cmp_eq_u32 %0, 1\n\ts_cbranch_scc1 1f\n\ts_waitcnt vmcnt(6)\n"
               "1:" :: "s"(lvl) : "scc", "memory");
}

constexpr int LDS_T = 0;
constexpr int LDS_ML = 4096;
constexpr int LDS_S = 4096 + 64;
constexpr int LDS_WAVE = LDS_S + TILES * 1024;
constexpr int OSTR = 132;
constexpr int LDS_KS = WAVES * LDS_WAVE;
constexpr int LDS_VS = LDS_KS + 8192;
constexpr int LDS_BYTES = LDS_VS + 8192;

constexpr unsigned WS_BLK = 4096;
constexpr u64 TAG_SALT = 0x9e3779b97f4a7c15ull;

__global__ void __launch_bounds__(NTHR, 1)
kivi_decode_attn(const unsigned short* __restrict__ q, const int* __restrict__ qk, const int* __restrict__ qv,
                 const unsigned short* __restrict__ ks, const unsigned short* __restrict__ kz,
                 const unsigned short* __restrict__ vs, const unsigned short* __restrict__ vz,
                 unsigned char* ws, unsigned* __restrict__ out) {
  unsigned tstart = (unsigned)__builtin_amdgcn_s_memrealtime();
  SB();
  __shared__ __attribute__((aligned(16))) unsigned char lds[LDS_BYTES];
  const int tid = threadIdx.x, lane = tid & 63, wave = __builtin_amdgcn_readfirstlane(tid >> 6);
  const int g = lane >> 4, c = lane & 15;
  const int bid = blockIdx.x;
  const int bh = ((bid >> 1) & 3) * 32 + (bid >> 3), split = bid & 1;
  const int key0 = split * KEYS_WG;
  const bool late = ((bid ^ (bid >> 3)) & 1) != 0;
  const int tau16 = late ? TAU_LATE16 : TAU_EARLY16, lvl_mid = late ? LVL_LATE : LVL_EARLY, sprint = late ? SPRINT_LATE : 2 * TILES - 2;
  int lvl = lvl_mid;
  auto pace = [&](int tiles_done) {
    const int lag = (int)((unsigned)__builtin_amdgcn_s_memrealtime() - tstart) - PACE_OFF - (((tiles_done + 1) * tau16) >> 4);
    const int l = lvl_mid + (lag > PACE_BAND ? -1 : (lag < -PACE_BAND ? 1 : 0));
    lvl = __builtin_amdgcn_readfirstlane(tiles_done >= sprint ? 0 : l);
  };

  const __amdgpu_buffer_rsrc_t rk = __builtin_amdgcn_make_buffer_rsrc((void*)qk, 0, KV_BYTES, 0x00020000);
  const __amdgpu_buffer_rsrc_t rv = __builtin_amdgcn_make_buffer_rsrc((void*)qv, 0, KV_BYTES, 0x00020000);
  const __amdgpu_buffer_rsrc_t rw = __builtin_amdgcn_make_buffer_rsrc((void*)ws, 0, NB * HKV * WAVES * WS_BLK, 0x00020000);
  const unsigned blk = (unsigned)(bh * WAVES + wave) * WS_BLK;
  const unsigned sbase = ((unsigned)bh * SKV + key0 + 16 * wave) * 512u;
  const unsigned voff = (unsigned)(4 * g) * 512u + (unsigned)c * 16u;
  v4u buf[16];
#define LOADKV(r, i, so) __builtin_amdgcn_raw_buffer_load_b128((r), voff + (i) * 256, (so), 2  )

  SB();
#pragma unroll
  for (int k = 0; k < KFIRST; ++k) { buf[k] = LOADKV(rk, k & 7, sbase + (k >> 3) * TSTRIDE); SB(); }
  v4u qf[4];
  {
    const unsigned short* qrow = q + ((size_t)bh * MROWS + (c & 7)) * D + 8 * g;
#pragma unroll
    for (int n = 0; n < 4; ++n) qf[n] = __builtin_nontemporal_load(reinterpret_cast<const v4u*>(qrow + 32 * n));
  }
  const size_t kso = ((size_t)bh * NG + split * (NG / NSPLIT)) * D + 4 * tid;
  const v2u ks_raw = __builtin_nontemporal_load(reinterpret_cast<const v2u*>(ks + kso));
  const v2u kz_raw = __builtin_nontemporal_load(reinterpret_cast<const v2u*>(kz + kso));
  const size_t vso = (size_t)bh * SKV + key0 + 4 * tid;
  const v2u vs_raw = __builtin_nontemporal_load(reinterpret_cast<const v2u*>(vs + vso));
  const v2u vz_raw = __builtin_nontemporal_load(reinterpret_cast<const v2u*>(vz + vso));
  gu64* ctl = (gu64*)(ws + blk);
  const u64 e_raw = __hip_atomic_load(ctl, __ATOMIC_RELAXED, __HIP_MEMORY_SCOPE_AGENT);
  SB();
#pragma unroll
  for (int k = KFIRST; k < 16; ++k) { buf[k] = LOADKV(rk, k & 7, sbase + (k >> 3) * TSTRIDE); SB(); }

  {
    f4 s, n;
    s[0] = bf_lo(ks_raw[0]); s[1] = bf_hi(ks_raw[0]); s[2] = bf_lo(ks_raw[1]); s[3] = bf_hi(ks_raw[1]);
    n[0] = -bf_lo(kz_raw[0]) * s[0]; n[1] = -bf_hi(kz_raw[0]) * s[1]; n[2] = -bf_lo(kz_raw[1]) * s[2]; n[3] = -bf_hi(kz_raw[1]) * s[3];
    *reinterpret_cast<f4*>(lds + LDS_KS + tid * 32) = s;
    *reinterpret_cast<f4*>(lds + LDS_KS + tid * 32 + 16) = n;
    s[0] = bf_lo(vs_raw[0]); s[1] = bf_hi(vs_raw[0]); s[2] = bf_lo(vs_raw[1]); s[3] = bf_hi(vs_raw[1]);
    n[0] = -bf_lo(vz_raw[0]) * s[0]; n[1] = -bf_hi(vz_raw[0]) * s[1]; n[2] = -bf_lo(vz_raw[1]) * s[2]; n[3] = -bf_hi(vz_raw[1]) * s[3];
    *reinterpret_cast<f4*>(lds + LDS_VS + tid * 32) = s;
    *reinterpret_cast<f4*>(lds + LDS_VS + tid * 32 + 16) = n;
  }
  const u64 epoch = rfl64(e_raw);
  __syncthreads();
  {
    const int late0 = (int)((unsigned)__builtin_amdgcn_s_memrealtime() - tstart) - PACE_ANCHOR;
    tstart += late0 > 0 ? late0 : 0;
  }

  unsigned char* wl = lds + wave * LDS_WAVE;
  constexpr float SCALE = 0.08838834764831845f;
  constexpr float LOG2E = 1.4426950408889634f;
  float m = -INFINITY;
#pragma unroll 1
  for (int it = 0; it < TILES / 2; ++it) {
    const bool last = it == TILES / 2 - 1;
    const __amdgpu_buffer_rsrc_t rn = last ? rv : rk;
    const unsigned son = sbase + (last ? 0u : (unsigned)(2 * it + 2) * TSTRIDE);
    const unsigned char* kls = lds + LDS_KS + (it * 32 + c) * 32;
    f4 sc[2], nz[2];
#pragma unroll
    for (int h = 0; h < 2; ++h) { sc[h] = *reinterpret_cast<const f4*>(kls + h * 512); nz[h] = *reinterpret_cast<const f4*>(kls + h * 512 + 16); }
#pragma unroll
    for (int u = 0; u < 2; ++u) {
#pragma unroll
      for (int i = 0; i < 8; ++i) {
        const int j = i >> 1, h = i & 1;
        const int r = 4 * g + j;
        float f[4];
#pragma unroll
        for (int e = 0; e < 4; ++e) f[e] = fmaf((float)(int)buf[8 * u + i][e], sc[h][e], nz[h][e]);
        v2u w; w[0] = pack_bf16(f[0], f[1]); w[1] = pack_bf16(f[2], f[3]);
        const int chunk = (8 * h + (c >> 1)) ^ r;
        *reinterpret_cast<v2u*>(wl + LDS_T + r * 256 + chunk * 16 + (c & 1) * 8) = w;
        SB();
        throttle(lvl);
        SB();
        buf[8 * u + i] = LOADKV(rn, i, son + u * TSTRIDE);
        SB();
      }
      pace(2 * it + u + 1);
      wave_sync();
      f4 acc = {0.f, 0.f, 0.f, 0.f};
#pragma unroll
      for (int n = 0; n < 4; ++n) {
        const v4u a = *reinterpret_cast<const v4u*>(wl + LDS_T + c * 256 + (((4 * n + g) ^ c) * 16));
        acc = __builtin_amdgcn_mfma_f32_16x16x32_bf16(__builtin_bit_cast(bf8v, a), __builtin_bit_cast(bf8v, qf[n]), acc, 0, 0, 0);
      }
#pragma unroll
      for (int r = 0; r < 4; ++r) { acc[r] = round_bf16(round_bf16(acc[r]) * SCALE) * LOG2E; m = fmaxf(m, acc[r]); }
      *reinterpret_cast<f4*>(wl + LDS_S + (2 * it + u) * 1024 + lane * 16) = acc;
      wave_sync();
    }
  }
  m = xg_max(m);
  float l = 0.f;
  f4 O[8];
#pragma unroll
  for (int k = 0; k < 8; ++k) O[k] = f4{0.f, 0.f, 0.f, 0.f};
  const int qr = 2 * wave + (lane >> 5), d0 = 4 * (lane & 31);
  const unsigned row = (unsigned)bh * MROWS + qr;
  const unsigned po_me = blk + 1024u + split * 1024u + lane * 16u, po_ot = blk + 1024u + (split ^ 1) * 1024u + lane * 16u;
  const unsigned ml_me = blk + 128u + split * 128u + (lane >> 5) * 8u, ml_ot = blk + 128u + (split ^ 1) * 128u + (lane >> 5) * 8u;
  const u64 tag_claim = mix64(epoch + TAG_SALT + 1), tag_pub = mix64(epoch + TAG_SALT + 2);
  v2u peek_raw = {0u, 0u};
  v4u po4; v2u pml2; bool have;
  auto vpass = [&](int it, bool last) {
    const unsigned son = sbase + (unsigned)(2 * it + 2) * TSTRIDE;
#pragma unroll
    for (int u = 0; u < 2; ++u) {
      const int t = 2 * it + u;
      const f4 sv = *reinterpret_cast<const f4*>(wl + LDS_S + t * 1024 + lane * 16);
      const unsigned char* vls = lds + LDS_VS + (t * 16 + 4 * wave + g) * 32;
      const f4 sc = *reinterpret_cast<const f4*>(vls), nz = *reinterpret_cast<const f4*>(vls + 16);
      float p[4];
#pragma unroll
      for (int r = 0; r < 4; ++r) { p[r] = __builtin_amdgcn_exp2f(sv[r] - m); l += p[r]; }
      v2u P; P[0] = pack_bf16(p[0], p[1]); P[1] = pack_bf16(p[2], p[3]);
      f4 fe[2];
      v2u a[8];
#pragma unroll
      for (int i = 0; i < 8; ++i) {
        const int j = i >> 1, h = i & 1;
        if (last && 8 * u + i == FETCH_STEP) {
          have = rfl64(((u64)peek_raw[1] << 32) | peek_raw[0]) == tag_pub;
          po4 = __builtin_amdgcn_raw_buffer_load_b128(rw, po_ot, have ? 0u : OOB, 16  );
          pml2 = __builtin_amdgcn_raw_buffer_load_b64(rw, ml_ot, have ? 0u : OOB, 16);
          SB();
        }
        f4 f;
#pragma unroll
        for (int e = 0; e < 4; ++e) f[e] = fmaf((float)(int)buf[8 * u + i][e], sc[j], nz[j]);
        if ((j & 1) == 0) { fe[h] = f; asm volatile("" : "+v"(fe[h])); }
        else {
#pragma unroll
          for (int e = 0; e < 4; ++e) { unsigned pk = pack_bf16(fe[h][e], f[e]); asm volatile("" : "+v"(pk)); a[h * 4 + e][j >> 1] = pk; }
        }
        SB();
        if (!last) {
          throttle(lvl);
          SB();
          buf[8 * u + i] = LOADKV(rv, i, son + u * TSTRIDE);
        }
        if (last && 8 * u + i == PEEK_STEP) peek_raw = __builtin_amdgcn_raw_buffer_load_b64(rw, 8, blk, 16);
        SB();
        if (j == 3) {
#pragma unroll
          for (int e = 0; e < 4; ++e)
            O[h * 4 + e] = __builtin_amdgcn_mfma_f32_16x16x16bf16_1k(__builtin_bit_cast(s4v, a[h * 4 + e]), __builtin_bit_cast(s4v, P), O[h * 4 + e], 0, 0, 0);
        }
      }
      if (!last) pace(TILES + t + 1);
    }
  };
#pragma unroll 1
  for (int it = 0; it < TILES / 2 - 1; ++it) vpass(it, false);
  vpass(TILES / 2 - 1, true);
  l = xg_sum(l);
  {
    float* ow = reinterpret_cast<float*>(wl + LDS_S) + (c & 7) * OSTR + (c >> 3) * 64 + 16 * g;
#pragma unroll
    for (int r = 0; r < 4; ++r) {
      f4 v;
#pragma unroll
      for (int e = 0; e < 4; ++e) v[e] = (c < 8) ? O[e][r] : O[4 + e][r];
      *reinterpret_cast<f4*>(ow + 4 * r) = v;
    }
    if (lane < 8) *reinterpret_cast<f2*>(wl + LDS_ML + lane * 8) = f2{m, l};
  }
  __syncthreads();
  float M = -INFINITY, L = 0.f;
  f4 o = {0.f, 0.f, 0.f, 0.f};
  {
    f2 ml[WAVES];
#pragma unroll
    for (int w = 0; w < WAVES; ++w) { ml[w] = *reinterpret_cast<const f2*>(lds + w * LDS_WAVE + LDS_ML + qr * 8); M = fmaxf(M, ml[w][0]); }
#pragma unroll
    for (int w = 0; w < WAVES; ++w) {
      const float wt = __builtin_amdgcn_exp2f(ml[w][0] - M);
      const f4 ov = *reinterpret_cast<const f4*>(lds + w * LDS_WAVE + LDS_S + (qr * OSTR + d0) * 4);
      L = fmaf(wt, ml[w][1], L);
#pragma unroll
      for (int e = 0; e < 4; ++e) o[e] = fmaf(wt, ov[e], o[e]);
    }
  }
  bool fin = true;
  if (!have) {
    __builtin_amdgcn_raw_buffer_store_b128(__builtin_bit_cast(v4u, o), rw, po_me, 0, 16);
    __builtin_amdgcn_raw_buffer_store_b64(__builtin_bit_cast(v2u, f2{M, L}), rw, (lane & 31) == 0 ? ml_me : OOB, 0, 16);
    u64 old = 0;
    if (lane == 0) old = __hip_atomic_exchange(ctl + 1, tag_claim, __ATOMIC_RELAXED, __HIP_MEMORY_SCOPE_AGENT);
    asm volatile("s_waitcnt vmcnt(0)" ::: "memory");
    old = rfl64(old);
    if (old == tag_claim || old == tag_pub) {
      for (int spin = 0; old != tag_pub && spin < (1 << 20); ++spin) {
        __builtin_amdgcn_s_sleep(2);
        old = rfl64(__hip_atomic_load(ctl + 1, __ATOMIC_RELAXED, __HIP_MEMORY_SCOPE_AGENT));
      }
      asm volatile("" ::: "memory");
      po4 = __builtin_amdgcn_raw_buffer_load_b128(rw, po_ot, 0, 16);
      pml2 = __builtin_amdgcn_raw_buffer_load_b64(rw, ml_ot, 0, 16);
    } else {
      if (lane == 0) __hip_atomic_store(ctl + 1, tag_pub, __ATOMIC_RELAXED, __HIP_MEMORY_SCOPE_AGENT);
      fin = false;
    }
  }
  if (fin) {
    const float M2 = __uint_as_float(pml2[0]), L2 = __uint_as_float(pml2[1]);
    const f4 o2 = __builtin_bit_cast(f4, po4);
    const float Ma = split ? M2 : M, La = split ? L2 : L, Mb = split ? M : M2, Lb = split ? L : L2;
    const float MM = fmaxf(Ma, Mb);
    const float wa = __builtin_amdgcn_exp2f(Ma - MM), wb = __builtin_amdgcn_exp2f(Mb - MM);
    const float inv = __builtin_amdgcn_rcpf(fmaf(wb, Lb, wa * La));
    float r[4];
#pragma unroll
    for (int e = 0; e < 4; ++e) { const float oa = split ? o2[e] : o[e], ob = split ? o[e] : o2[e]; r[e] = fmaf(wb, ob, wa * oa) * inv; }
    v2u w; w[0] = pack_bf16(r[0], r[1]); w[1] = pack_bf16(r[2], r[3]);
    *reinterpret_cast<v2u*>(out + (size_t)row * (D / 2) + d0 / 2) = w;
    if (lane == 0) __hip_atomic_store(ctl, epoch + 2, __ATOMIC_RELAXED, __HIP_MEMORY_SCOPE_AGENT);
  }
}
}

extern "C" void kernel_launch(void* const* d_in, const int* in_sizes, int n_in, void* d_out, int out_size, void* d_ws, size_t ws_size,
                              hipStream_t stream) {
  (void)in_sizes; (void)n_in; (void)out_size; (void)ws_size;
  kivi_decode_attn<<<NB * HKV * NSPLIT, NTHR, 0, stream>>>(
      (const unsigned short*)d_in[0], (const int*)d_in[1], (const int*)d_in[2], (const unsigned short*)d_in[3],
      (const unsigned short*)d_in[4], (const unsigned short*)d_in[5], (const unsigned short*)d_in[6],
      (unsigned char*)d_ws, (unsigned*)d_out);
}
```

```cpp
#include <hip/hip_runtime.h>
#include <hip/hip_bf16.h>
#include <stdint.h>

namespace {
constexpr int NB = 8, SQ = 4, SKV = 2048, HQ = 32, HKV = 16, D = 128, NG = 16;
constexpr int MROWS = (HQ / HKV) * SQ;
constexpr int WAVES = 4, NTHR = WAVES * 64;
constexpr int NSPLIT = 2;
constexpr int KEYS_WG = SKV / NSPLIT;
constexpr int TILES = KEYS_WG / (16 * WAVES);
constexpr unsigned KV_BYTES = (unsigned)NB * HKV * SKV * D * 4u;
constexpr unsigned OOB = 0x80000000u;
constexpr unsigned TSTRIDE = WAVES * 16 * 512;
static_assert(MROWS == 8 && TILES == 16 && SKV / NG == 128, "layout below is written for these shapes");

constexpr int TAU_EARLY16 = 1616, TAU_LATE16 = 1864;
constexpr int PACE_OFF = 220, PACE_BAND = 30, PACE_ANCHOR = 230;
constexpr int LVL_EARLY = 2, LVL_LATE = 3;
constexpr int SPRINT_LATE = 26;
constexpr int KFIRST = 4;
constexpr int PEEK_STEP = 4, FETCH_STEP = 15;

typedef unsigned v4u __attribute__((ext_vector_type(4)));
typedef unsigned v2u __attribute__((ext_vector_type(2)));
typedef float f4 __attribute__((ext_vector_type(4)));
typedef float f2 __attribute__((ext_vector_type(2)));
typedef short s4v __attribute__((ext_vector_type(4)));
typedef __bf16 bf8v __attribute__((ext_vector_type(8)));
typedef __bf16 bf2v __attribute__((ext_vector_type(2)));
typedef unsigned long long u64;
typedef __attribute__((address_space(1))) u64 gu64;

__device__ __forceinline__ unsigned pack_bf16(float lo, float hi) {
  bf2v v; v[0] = (__bf16)lo; v[1] = (__bf16)hi;
  return __builtin_bit_cast(unsigned, v);
}
__device__ __forceinline__ float bf_lo(unsigned u) { return __uint_as_float(u << 16); }
__device__ __forceinline__ float bf_hi(unsigned u) { return __uint_as_float(u & 0xffff0000u); }
__device__ __forceinline__ float round_bf16(float x) { return bf_lo(pack_bf16(x, x)); }

__device__ __forceinline__ f4 dequant4(v4u x, f4 z, f4 s) {
  const unsigned p0 = pack_bf16((float)(int)x[0] - z[0], (float)(int)x[1] - z[1]);
  const unsigned p1 = pack_bf16((float)(int)x[2] - z[2], (float)(int)x[3] - z[3]);
  return f4{bf_lo(p0) * s[0], bf_hi(p0) * s[1], bf_lo(p1) * s[2], bf_hi(p1) * s[3]};
}

__device__ __forceinline__ float xg_max(float x) {
  auto s = __builtin_amdgcn_permlane16_swap(__float_as_uint(x), __float_as_uint(x), false, false);
  x = fmaxf(__uint_as_float(s[0]), __uint_as_float(s[1]));
  auto t = __builtin_amdgcn_permlane32_swap(__float_as_uint(x), __float_as_uint(x), false, false);
  return fmaxf(__uint_as_float(t[0]), __uint_as_float(t[1]));
}
__device__ __forceinline__ float xg_sum(float x) {
  auto s = __builtin_amdgcn_permlane16_swap(__float_as_uint(x), __float_as_uint(x), false, false);
  x = __uint_as_float(s[0]) + __uint_as_float(s[1]);
  auto t = __builtin_amdgcn_permlane32_swap(__float_as_uint(x), __float_as_uint(x), false, false);
  return __uint_as_float(t[0]) + __uint_as_float(t[1]);
}
__device__ __forceinline__ void wave_sync() {
  __builtin_amdgcn_fence(__ATOMIC_RELEASE, "wavefront");
  __builtin_amdgcn_wave_barrier();
  __builtin_amdgcn_fence(__ATOMIC_ACQUIRE, "wavefront");
}
__device__ __forceinline__ u64 rfl64(u64 x) {
  const unsigned lo = __builtin_amdgcn_readfirstlane((unsigned)x), hi = __builtin_amdgcn_readfirstlane((unsigned)(x >> 32));
  return ((u64)hi << 32) | lo;
}
__device__ __forceinline__ u64 mix64(u64 z) {
  z = (z ^ (z >> 30)) * 0xbf58476d1ce4e5b9ull;
  z = (z ^ (z >> 27)) * 0x94d049bb133111ebull;
  return z ^ (z >> 31);
}
#define SB() __builtin_amdgcn_sched_barrier(0)
__device__ __forceinline__ void throttle(int lvl) {
  asm volatile("s_cmp_eq_u32 %0, 0\n\ts_cbranch_scc1 1f\n\t"
               "s_waitcnt vmcnt(7)\n\ts_cmp_eq_u32 %0, 1\n\ts_cbranch_scc1 1f\n\t"
               "s_waitcnt vmcnt(6)\n\ts_cmp_eq_u32 %0, 2\n\ts_cbranch_scc1 1f\n\t"
               "s_waitcnt vmcnt(5)\n\ts_cmp_eq_u32 %0, 3\n\ts_cbranch_scc1 1f\n\t"
               "s_waitcnt vmcnt(4)\n"
               "1:" :: "s"(lvl) : "scc", "memory");
}

constexpr int LDS_T = 0;
constexpr int LDS_ML = 4096;
constexpr int LDS_S = 4096 + 64;
constexpr int LDS_WAVE = LDS_S + TILES * 512;
constexpr int OSTR = 132;
constexpr int LDS_KS = WAVES * LDS_WAVE;
constexpr int LDS_VS = LDS_KS + 8192;
constexpr int LDS_BYTES = LDS_VS + 8192;

constexpr unsigned WS_BLK = 4096;
constexpr u64 TAG_SALT = 0x9e3779b97f4a7c15ull;

__global__ void __launch_bounds__(NTHR, 2)
kivi_decode_attn(const unsigned short* __restrict__ q, const int* __restrict__ qk, const int* __restrict__ qv,
                 const unsigned short* __restrict__ ks, const unsigned short* __restrict__ kz,
                 const unsigned short* __restrict__ vs, const unsigned short* __restrict__ vz,
                 unsigned char* ws, unsigned* __restrict__ out) {
  unsigned tstart = (unsigned)__builtin_amdgcn_s_memrealtime();
  SB();
  __shared__ __attribute__((aligned(16))) unsigned char lds[LDS_BYTES];
  const int tid = threadIdx.x, lane = tid & 63, wave = __builtin_amdgcn_readfirstlane(tid >> 6);
  const int g = lane >> 4, c = lane & 15;
  const int bid = blockIdx.x;
  const int bh = ((bid >> 1) & 3) * 32 + (bid >> 3), split = bid & 1;
  const int key0 = split * KEYS_WG;
  const bool late = ((bid ^ (bid >> 3)) & 1) != 0;
  const int tau16 = late ? TAU_LATE16 : TAU_EARLY16, lvl_mid = late ? LVL_LATE : LVL_EARLY, sprint = late ? SPRINT_LATE : 2 * TILES - 2;
  int lvl = lvl_mid;
  auto pace = [&](int tiles_done) {
    const int lag = (int)((unsigned)__builtin_amdgcn_s_memrealtime() - tstart) - PACE_OFF - (((tiles_done + 1) * tau16) >> 4);
    const int l = lvl_mid + (lag > PACE_BAND ? -1 : (lag < -PACE_BAND ? 1 : 0));
    lvl = __builtin_amdgcn_readfirstlane(tiles_done >= sprint ? 0 : l);
  };

  const __amdgpu_buffer_rsrc_t rk = __builtin_amdgcn_make_buffer_rsrc((void*)qk, 0, KV_BYTES, 0x00020000);
  const __amdgpu_buffer_rsrc_t rv = __builtin_amdgcn_make_buffer_rsrc((void*)qv, 0, KV_BYTES, 0x00020000);
  const __amdgpu_buffer_rsrc_t rw = __builtin_amdgcn_make_buffer_rsrc((void*)ws, 0, NB * HKV * WAVES * WS_BLK, 0x00020000);
  const unsigned blk = (unsigned)(bh * WAVES + wave) * WS_BLK;
  const unsigned sbase = ((unsigned)bh * SKV + key0 + 16 * wave) * 512u;
  const unsigned voff = (unsigned)(4 * g) * 512u + (unsigned)c * 16u;
  v4u buf[16];
#define LOADKV(r, i, so) __builtin_amdgcn_raw_buffer_load_b128((r), voff + (i) * 256, (so), 2  )

  SB();
#pragma unroll
  for (int k = 0; k < KFIRST; ++k) { buf[k] = LOADKV(rk, k & 7, sbase + (k >> 3) * TSTRIDE); SB(); }
  v4u qf[4];
  {
    const unsigned short* qrow = q + ((size_t)bh * MROWS + (c & 7)) * D + 8 * g;
#pragma unroll
    for (int n = 0; n < 4; ++n) qf[n] = __builtin_nontemporal_load(reinterpret_cast<const v4u*>(qrow + 32 * n));
  }
  const size_t kso = ((size_t)bh * NG + split * (NG / NSPLIT)) * D + 4 * tid;
  const v2u ks_raw = __builtin_nontemporal_load(reinterpret_cast<const v2u*>(ks + kso));
  const v2u kz_raw = __builtin_nontemporal_load(reinterpret_cast<const v2u*>(kz + kso));
  const size_t vso = (size_t)bh * SKV + key0 + 4 * tid;
  const v2u vs_raw = __builtin_nontemporal_load(reinterpret_cast<const v2u*>(vs + vso));
  const v2u vz_raw = __builtin_nontemporal_load(reinterpret_cast<const v2u*>(vz + vso));
  gu64* ctl = (gu64*)(ws + blk);
  const u64 e_raw = __hip_atomic_load(ctl, __ATOMIC_RELAXED, __HIP_MEMORY_SCOPE_AGENT);
  SB();
#pragma unroll
  for (int k = KFIRST; k < 16; ++k) { buf[k] = LOADKV(rk, k & 7, sbase + (k >> 3) * TSTRIDE); SB(); }

  {
    *reinterpret_cast<f4*>(lds + LDS_KS + tid * 32) = f4{bf_lo(ks_raw[0]), bf_hi(ks_raw[0]), bf_lo(ks_raw[1]), bf_hi(ks_raw[1])};
    *reinterpret_cast<f4*>(lds + LDS_KS + tid * 32 + 16) = f4{bf_lo(kz_raw[0]), bf_hi(kz_raw[0]), bf_lo(kz_raw[1]), bf_hi(kz_raw[1])};
    const f4 s = {bf_lo(vs_raw[0]), bf_hi(vs_raw[0]), bf_lo(vs_raw[1]), bf_hi(vs_raw[1])};
    *reinterpret_cast<f4*>(lds + LDS_VS + tid * 32) = s;
    *reinterpret_cast<f4*>(lds + LDS_VS + tid * 32 + 16) = f4{-bf_lo(vz_raw[0]) * s[0], -bf_hi(vz_raw[0]) * s[1], -bf_lo(vz_raw[1]) * s[2], -bf_hi(vz_raw[1]) * s[3]};
  }
  const u64 epoch = rfl64(e_raw);
  __syncthreads();
  {
    const int late0 = (int)((unsigned)__builtin_amdgcn_s_memrealtime() - tstart) - PACE_ANCHOR;
    tstart += late0 > 0 ? late0 : 0;
  }

  unsigned char* wl = lds + wave * LDS_WAVE;
  const int soff = g * 128 + (c & 7) * 16;
  constexpr float SCALE = 0.08838834764831845f;
  constexpr float LOG2E = 1.4426950408889634f;
  float m = -INFINITY;
#pragma unroll 1
  for (int it = 0; it < TILES / 2; ++it) {
    const bool last = it == TILES / 2 - 1;
    const __amdgpu_buffer_rsrc_t rn = last ? rv : rk;
    const unsigned son = sbase + (last ? 0u : (unsigned)(2 * it + 2) * TSTRIDE);
    const unsigned char* kls = lds + LDS_KS + (it * 32 + c) * 32;
    f4 sc[2], zr[2];
#pragma unroll
    for (int h = 0; h < 2; ++h) { sc[h] = *reinterpret_cast<const f4*>(kls + h * 512); zr[h] = *reinterpret_cast<const f4*>(kls + h * 512 + 16); }
#pragma unroll
    for (int u = 0; u < 2; ++u) {
#pragma unroll
      for (int i = 0; i < 8; ++i) {
        const int j = i >> 1, h = i & 1;
        const int r = 4 * g + j;
        const f4 f = dequant4(buf[8 * u + i], zr[h], sc[h]);
        v2u w; w[0] = pack_bf16(f[0], f[1]); w[1] = pack_bf16(f[2], f[3]);
        const int chunk = (8 * h + (c >> 1)) ^ r;
        *reinterpret_cast<v2u*>(wl + LDS_T + r * 256 + chunk * 16 + (c & 1) * 8) = w;
        SB();
        throttle(lvl);
        SB();
        buf[8 * u + i] = LOADKV(rn, i, son + u * TSTRIDE);
        SB();
      }
      pace(2 * it + u + 1);
      wave_sync();
      f4 acc = {0.f, 0.f, 0.f, 0.f};
#pragma unroll
      for (int n = 0; n < 4; ++n) {
        const v4u a = *reinterpret_cast<const v4u*>(wl + LDS_T + c * 256 + (((4 * n + g) ^ c) * 16));
        acc = __builtin_amdgcn_mfma_f32_16x16x32_bf16(__builtin_bit_cast(bf8v, a), __builtin_bit_cast(bf8v, qf[n]), acc, 0, 0, 0);
      }
#pragma unroll
      for (int r = 0; r < 4; ++r) { acc[r] = round_bf16(round_bf16(acc[r]) * SCALE) * LOG2E; m = fmaxf(m, acc[r]); }
      *reinterpret_cast<f4*>(wl + LDS_S + (2 * it + u) * 512 + soff) = acc;
      wave_sync();
    }
  }
  u64 tag_claim = mix64(epoch + TAG_SALT + 1), tag_pub = mix64(epoch + TAG_SALT + 2);
  asm volatile("" : "+s"(tag_claim), "+s"(tag_pub));
  m = xg_max(m);
  float l = 0.f;
  f4 O[8];
#pragma unroll
  for (int k = 0; k < 8; ++k) O[k] = f4{0.f, 0.f, 0.f, 0.f};
  const int qr = 2 * wave + (lane >> 5), d0 = 4 * (lane & 31);
  const unsigned row = (unsigned)bh * MROWS + qr;
  const unsigned po_me = blk + 1024u + split * 1024u + lane * 16u, po_ot = blk + 1024u + (split ^ 1) * 1024u + lane * 16u;
  const unsigned ml_me = blk + 128u + split * 128u + (lane >> 5) * 8u, ml_ot = blk + 128u + (split ^ 1) * 128u + (lane >> 5) * 8u;
  v2u peek_raw = {0u, 0u};
  v4u po4; v2u pml2; bool have;
  auto vpass = [&](int it, bool last) {
    const unsigned son = sbase + (unsigned)(2 * it + 2) * TSTRIDE;
#pragma unroll
    for (int u = 0; u < 2; ++u) {
      const int t = 2 * it + u;
      const f4 sv = *reinterpret_cast<const f4*>(wl + LDS_S + t * 512 + soff);
      const unsigned char* vls = lds + LDS_VS + (t * 16 + 4 * wave + g) * 32;
      const f4 sc = *reinterpret_cast<const f4*>(vls), nzs = *reinterpret_cast<const f4*>(vls + 16);
      float p[4];
#pragma unroll
      for (int r = 0; r < 4; ++r) { p[r] = __builtin_amdgcn_exp2f(sv[r] - m); l += p[r]; }
      v2u P; P[0] = pack_bf16(p[0], p[1]); P[1] = pack_bf16(p[2], p[3]);
      f4 fe[2];
      v2u a[8];
#pragma unroll
      for (int i = 0; i < 8; ++i) {
        const int j = i >> 1, h = i & 1;
        if (last && 8 * u + i == FETCH_STEP) {
          have = rfl64(((u64)peek_raw[1] << 32) | peek_raw[0]) == tag_pub;
          po4 = __builtin_amdgcn_raw_buffer_load_b128(rw, po_ot, have ? 0u : OOB, 16  );
          pml2 = __builtin_amdgcn_raw_buffer_load_b64(rw, ml_ot, have ? 0u : OOB, 16);
          SB();
        }
        f4 f;
#pragma unroll
        for (int e = 0; e < 4; ++e) f[e] = fmaf((float)(int)buf[8 * u + i][e], sc[j], nzs[j]);
        asm volatile("" : "+v"(f));
        if ((j & 1) == 0) fe[h] = f;
        else {
#pragma unroll
          for (int e = 0; e < 4; ++e) a[h * 4 + e][j >> 1] = pack_bf16(fe[h][e], f[e]);
        }
        SB();
        if (!last) {
          throttle(lvl);
          SB();
          buf[8 * u + i] = LOADKV(rv, i, son + u * TSTRIDE);
        }
        if (last && 8 * u + i == PEEK_STEP) peek_raw = __builtin_amdgcn_raw_buffer_load_b64(rw, 8, blk, 16);
        SB();
        if (j == 3) {
#pragma unroll
          for (int e = 0; e < 4; ++e)
            O[h * 4 + e] = __builtin_amdgcn_mfma_f32_16x16x16bf16_1k(__builtin_bit_cast(s4v, a[h * 4 + e]), __builtin_bit_cast(s4v, P), O[h * 4 + e], 0, 0, 0);
        }
      }
      if (!last) pace(TILES + t + 1);
    }
  };
#pragma unroll 1
  for (int it = 0; it < TILES / 2 - 1; ++it) vpass(it, false);
  vpass(TILES / 2 - 1, true);
  l = xg_sum(l);
  {
    float* ow = reinterpret_cast<float*>(wl + LDS_S) + (c & 7) * OSTR + (c >> 3) * 64 + 16 * g;
#pragma unroll
    for (int r = 0; r < 4; ++r) {
      f4 v;
#pragma unroll
      for (int e = 0; e < 4; ++e) v[e] = (c < 8) ? O[e][r] : O[4 + e][r];
      *reinterpret_cast<f4*>(ow + 4 * r) = v;
    }
    if (lane < 8) *reinterpret_cast<f2*>(wl + LDS_ML + lane * 8) = f2{m, l};
  }
  __syncthreads();
  float M = -INFINITY, L = 0.f;
  f4 o = {0.f, 0.f, 0.f, 0.f};
  {
    f2 ml[WAVES];
#pragma unroll
    for (int w = 0; w < WAVES; ++w) { ml[w] = *reinterpret_cast<const f2*>(lds + w * LDS_WAVE + LDS_ML + qr * 8); M = fmaxf(M, ml[w][0]); }
#pragma unroll
    for (int w = 0; w < WAVES; ++w) {
      const float wt = __builtin_amdgcn_exp2f(ml[w][0] - M);
      const f4 ov = *reinterpret_cast<const f4*>(lds + w * LDS_WAVE + LDS_S + (qr * OSTR + d0) * 4);
      L = fmaf(wt, ml[w][1], L);
#pragma unroll
      for (int e = 0; e < 4; ++e) o[e] = fmaf(wt, ov[e], o[e]);
    }
  }
  bool fin = true;
  if (!have) {
    __builtin_amdgcn_raw_buffer_store_b128(__builtin_bit_cast(v4u, o), rw, po_me, 0, 16);
    __builtin_amdgcn_raw_buffer_store_b64(__builtin_bit_cast(v2u, f2{M, L}), rw, (lane & 31) == 0 ? ml_me : OOB, 0, 16);
    u64 old = 0;
    if (lane == 0) old = __hip_atomic_exchange(ctl + 1, tag_claim, __ATOMIC_RELAXED, __HIP_MEMORY_SCOPE_AGENT);
    asm volatile("s_waitcnt vmcnt(0)" ::: "memory");
    old = rfl64(old);
    if (old == tag_claim || old == tag_pub) {
      for (int spin = 0; old != tag_pub && spin < (1 << 20); ++spin) {
        __builtin_amdgcn_s_sleep(2);
        old = rfl64(__hip_atomic_load(ctl + 1, __ATOMIC_RELAXED, __HIP_MEMORY_SCOPE_AGENT));
      }
      asm volatile("" ::: "memory");
      po4 = __builtin_amdgcn_raw_buffer_load_b128(rw, po_ot, 0, 16);
      pml2 = __builtin_amdgcn_raw_buffer_load_b64(rw, ml_ot, 0, 16);
    } else {
      if (lane == 0) __hip_atomic_store(ctl + 1, tag_pub, __ATOMIC_RELAXED, __HIP_MEMORY_SCOPE_AGENT);
      fin = false;
    }
  }
  if (fin) {
    const float M2 = __uint_as_float(pml2[0]), L2 = __uint_as_float(pml2[1]);
    const f4 o2 = __builtin_bit_cast(f4, po4);
    const float Ma = split ? M2 : M, La = split ? L2 : L, Mb = split ? M : M2, Lb = split ? L : L2;
    const float MM = fmaxf(Ma, Mb);
    const float wa = __builtin_amdgcn_exp2f(Ma - MM), wb = __builtin_amdgcn_exp2f(Mb - MM);
    const float inv = __builtin_amdgcn_rcpf(fmaf(wb, Lb, wa * La));
    float r[4];
#pragma unroll
    for (int e = 0; e < 4; ++e) { const float oa = split ? o2[e] : o[e], ob = split ? o[e] : o2[e]; r[e] = fmaf(wb, ob, wa * oa) * inv; }
    v2u w; w[0] = pack_bf16(r[0], r[1]); w[1] = pack_bf16(r[2], r[3]);
    const __amdgpu_buffer_rsrc_t ro = __builtin_amdgcn_make_buffer_rsrc((void*)out, 0, NB * HQ * SQ * D * 2, 0x00020000);
    __builtin_amdgcn_raw_buffer_store_b64(w, ro, row * (D * 2) + d0 * 2, 0, 16);
    if (lane == 0) __hip_atomic_store(ctl, epoch + 2, __ATOMIC_RELAXED, __HIP_MEMORY_SCOPE_AGENT);
  }
}
}

extern "C" void kernel_launch(void* const* d_in, const int* in_sizes, int n_in, void* d_out, int out_size, void* d_ws, size_t ws_size,
                              hipStream_t stream) {
  (void)in_sizes; (void)n_in; (void)out_size; (void)ws_size;
  kivi_decode_attn<<<NB * HKV * NSPLIT, NTHR, 0, stream>>>(
      (const unsigned short*)d_in[0], (const int*)d_in[1], (const int*)d_in[2], (const unsigned short*)d_in[3],
      (const unsigned short*)d_in[4], (const unsigned short*)d_in[5], (const unsigned short*)d_in[6],
      (unsigned char*)d_ws, (unsigned*)d_out);
}
```
